```python
import torch

def _softmax(x, axis=-1):
    return torch.softmax(x, dim=axis)

def _gelu(x):
    return torch.nn.functional.gelu(x, approximate='tanh')

def _silu(x):
    return torch.nn.functional.silu(x)

def _sigmoid(x):
    return torch.sigmoid(x)

def _layernorm(x, axis=-1, eps=1e-05):
    m = x.mean(dim=axis, keepdim=True)
    v = x.var(dim=axis, keepdim=True, unbiased=False)
    return (x - m) / torch.sqrt(v + eps)

def _scatter_add(tgt, idx, src, axis=0):
    return tgt.clone().index_add_(axis, idx, src)

def _segment_sum(src, seg, n):
    out = torch.zeros((n,) + src.shape[1:], dtype=src.dtype)
    return out.index_add_(0, seg, src)

def _pad(x, pw):
    flat = tuple((v for pair in reversed(pw) for v in pair))
    return torch.nn.functional.pad(x, flat)
kernel_name = 'interleave_layers__ssm_conv_h3__memory_aug_recurrent_mem_tr__prompt'

def setup_inputs():

    _w = lambda a: (lambda t: t.to(torch.bfloat16) if t.is_floating_point() else t)(torch.as_tensor(a))

    def _A_setup():
        B, S, D = (1, 2048, 512)
        ks = 4
        return {'q': _w((lambda *s: torch.randn(*s, dtype=torch.bfloat16) * 0.05)(B, S, D)), 'k': _w((lambda *s: torch.randn(*s, dtype=torch.bfloat16) * 0.2)(B, S, D)), 'v': _w((lambda *s: torch.randn(*s, dtype=torch.bfloat16) * 0.2)(B, S, D)), 'shift_k': _w((lambda *s: torch.randn(*s, dtype=torch.bfloat16) * 0.2)(D, ks) * 0.5), 'ssm_k': _w((lambda *s: torch.randn(*s, dtype=torch.bfloat16) * 0.4)(D, S) * (1.0 / S ** 0.5)), 'ssm_D': _w((lambda *s: torch.randn(*s, dtype=torch.bfloat16) * 0.2)(D))}
    _A_in = _A_setup()

    def _B_setup():
        B, S, Dm = (1, 2048, 512)
        H, Dh = (32, 128)
        M, seg = (8, 32)
        seg = min(seg, S)
        S = S // seg * seg
        return {'x': _w((lambda *s: torch.randn(*s, dtype=torch.bfloat16) * 0.05)(B, S, Dm)), 'mem0': _w((lambda *s: torch.randn(*s, dtype=torch.bfloat16) * 0.2)(B, M, Dm)), 'Wq': _w((lambda *s: torch.randn(*s, dtype=torch.bfloat16) * 1.6)(Dm, H, Dh) * Dm ** (-0.5)), 'Wk': _w((lambda *s: torch.randn(*s, dtype=torch.bfloat16) * 1.6)(Dm, H, Dh) * Dm ** (-0.5)), 'Wv': _w((lambda *s: torch.randn(*s, dtype=torch.bfloat16) * 0.2)(Dm, H, Dh) * Dm ** (-0.5)), 'Wo': _w((lambda *s: torch.randn(*s, dtype=torch.bfloat16) * 0.2)(H, Dh, Dm) * (H * Dh) ** (-0.5)), 'seg_len': seg}
    _B_in = _B_setup()
    return {'A_q': _A_in['q'], 'A_k': _A_in['k'], 'A_v': _A_in['v'], 'A_shift_k': _A_in['shift_k'], 'A_ssm_k': _A_in['ssm_k'], 'A_ssm_D': _A_in['ssm_D'], 'B_x': _B_in['x'], 'B_mem0': _B_in['mem0'], 'B_Wq': _B_in['Wq'], 'B_Wk': _B_in['Wk'], 'B_Wv': _B_in['Wv'], 'B_Wo': _B_in['Wo'], 'B_seg_len': _B_in['seg_len']}

def reference(A_q, A_k, A_v, A_shift_k, A_ssm_k, A_ssm_D, B_x, B_mem0, B_Wq, B_Wk, B_Wv, B_Wo, B_seg_len):

    def _A_ref(q, k, v, shift_k, ssm_k, ssm_D):
        B, S, D = q.shape
        ks = shift_k.shape[-1]
        k_pad = _pad(k, ((0, 0), (ks - 1, 0), (0, 0)))
        k_shift = torch.zeros_like(k)
        for t in range(ks):
            k_shift = k_shift + k_pad[:, t:t + S, :] * shift_k[:, ks - 1 - t][None, None, :]
        kv = k_shift * v
        L = 2 * S
        kv_pad = _pad(kv, ((0, 0), (0, S), (0, 0)))
        kf_pad = _pad(ssm_k, ((0, 0), (0, S)))
        kv_f = (lambda x, axes=(-2, -1): torch.fft.rfft2(x.float(), dim=axes))(kv_pad[:, None, :, :], axes=(1, 2))
        kf_f = (lambda x, axes=(-2, -1): torch.fft.rfft2(x.float(), dim=axes))(kf_pad[:, None, :], axes=(1, 2))
        kf_f = (lambda x, *d: x.permute(*d))(kf_f, 1, 2, 0)
        y_f = kv_f * kf_f[None, :, :, :]
        y = (lambda x, s, axes=(-2, -1): torch.fft.irfft2(x, s=s, dim=axes).to(torch.bfloat16))(y_f, (1, L), axes=(1, 2))[:, 0, :S, :]
        y = y + kv * ssm_D[None, None, :]
        return q * y

    def _B_ref(x, mem0, Wq, Wk, Wv, Wo, seg_len):
        B, S, Dm = x.shape
        H, Dh = (Wq.shape[1], Wq.shape[2])
        M = mem0.shape[1]
        scale = Dh ** (-0.5)
        n_seg = S // seg_len
        L = M + seg_len
        idx = torch.arange(L)
        mask = (idx[:, None] < M) | (idx[:, None] >= idx[None, :])
        mem = mem0
        outs = []
        for s in range(n_seg):
            xs = x[:, s * seg_len:(s + 1) * seg_len, :].clone()
            h = torch.concatenate([mem, xs], axis=1)
            q = torch.einsum('btd,dhe->bhte', h, Wq)
            k = torch.einsum('btd,dhe->bhte', h, Wk)
            v = torch.einsum('btd,dhe->bhte', h, Wv)
            scores = torch.einsum('bhqe,bhke->bhqk', q, k) * scale
            scores = torch.where(mask[None, None], scores, -1000000000.0)
            p = _softmax(scores, axis=-1)
            o = torch.einsum('bhqk,bhke->bhqe', p, v)
            o = torch.einsum('bhte,hed->btd', o, Wo)
            mem = o[:, :M, :].clone()
            outs.append(o[:, M:, :])
        return torch.concatenate(outs, axis=1)
    x = A_q
    x = x + _A_ref(_layernorm(x, axis=-1), A_k, A_v, A_shift_k, A_ssm_k, A_ssm_D)
    x = x + _B_ref(_layernorm(x, axis=-1), B_mem0, B_Wq, B_Wk, B_Wv, B_Wo, B_seg_len)
    x = x + _A_ref(_layernorm(x, axis=-1), A_k, A_v, A_shift_k, A_ssm_k, A_ssm_D)
    x = x + _B_ref(_layernorm(x, axis=-1), B_mem0, B_Wq, B_Wk, B_Wv, B_Wo, B_seg_len)
    return x
```

```cpp
#include <hip/hip_runtime.h>
#include <hip/hip_bf16.h>

#define MB (1u << 20)
#define WS_MP    (0u * MB)
#define WS_IMG   (2u * MB)
#define WS_FLG   (2u * MB + 524288u)
#define WS_HU    (3u * MB + 524288u)
#define WS_H     (4u * MB)
#define WS_TP    (8u * MB)
#define WS_YT    (16u * MB)
#define WS_X3    (20u * MB)
#define WS_X1    (22u * MB)
#define WS_Y     (24u * MB)
#define WS_CTL   (26u * MB)
#define WS_CST   (WS_CTL + 32768u)
#define WS_PAR   (WS_CTL + 65536u)
#define WS_KV    (28u * MB)
#define WS_DBG   (44u * MB)
#define WS_END   (52u * MB)

typedef unsigned short u16;
typedef unsigned v4u __attribute__((ext_vector_type(4)));
typedef float v16f __attribute__((ext_vector_type(16)));
typedef __bf16 v8b __attribute__((ext_vector_type(8)));

__device__ __forceinline__ float bf2f(unsigned h) { return __uint_as_float(h << 16); }
__device__ __forceinline__ unsigned f2bf(float f) {
  unsigned u = __float_as_uint(f);
  return (f != f) ? 0x7fc0u : ((u + 0x7fffu + ((u >> 16) & 1u)) >> 16);
}
__device__ __forceinline__ unsigned cbf(float f) { return __builtin_bit_cast(u16, (__bf16)f); }
__device__ __forceinline__ float rbf(float f) { return bf2f(cbf(f)); }

__device__ __forceinline__ void st16(char* p, v4u v) {
  asm volatile("global_store_dwordx4 %0, %1, off sc1\n\ts_nop 1" : : "v"(p), "v"(v) : "memory");
}

__device__ __forceinline__ void touch_code(unsigned off, unsigned sink) {
  v4u srd;
  asm volatile("s_getpc_b64 s[92:93]\n"
               "Ltouch%=:\n"
               "s_and_b32 s93, s93, 0xffff\n"
               "s_mov_b32 s94, Lcode_end-Ltouch%=\n"
               "s_mov_b32 s95, 0x20000\n"
               "s_mov_b32 m0, %1\n"
               "s_nop 0\n"
               "buffer_load_dword %0, s[92:95], 0 offen lds\n"
               : : "v"(off), "s"(sink) : "s92", "s93", "s94", "s95", "scc", "memory");
}

template <int N, int K = 63>
__device__ __forceinline__ void touch(const void* base, unsigned off, unsigned step, unsigned sink) {
  const v4u srd = {(unsigned)(size_t)base, (unsigned)((size_t)base >> 32) & 0xffffu, 4 * MB, 0x20000u};
#pragma unroll
  for (int i = 0; i < N; ++i)
    asm volatile("s_mov_b32 m0, %3\n\ts_nop 4\n\tbuffer_load_dword %0, %1, %2 offen lds\n\ts_waitcnt vmcnt(%4)"
                 : : "v"(off), "s"(srd), "s"(i * step), "s"(sink), "n"(K) : "memory");
}

#define PAR(i, v) asm("v_writelane_b32 %0, %1, " #i : "+v"(par) : "s"(v))
__device__ __forceinline__ void params(char* ws, const u16* xin, u16* out, unsigned q) {
  const unsigned lane = q & 63, c = lane & 15, g = lane >> 4;
  const unsigned w = __builtin_amdgcn_readfirstlane(q >> 6);
  const unsigned x = blockIdx.x & 7, y = blockIdx.x >> 3;
  const unsigned L = x >> 2, r = y & 3, h = (x & 3) * 8 + (y >> 2);
  auto near = [](unsigned xx, unsigned k) { return ((((xx & 1) << 2 | (xx >> 2) << 1 | ((xx >> 1) & 1)) ^ k) & 7) << 12; };
  const unsigned cm = ((r & 1) << 2) ^ (r >> 1);
  v4u t0, t1;
  {
    const unsigned one = 0x3f80u << (16 * (c & 1));
    if (w == 0) {
      t0 = (v4u){0, 0, 0, 0};
      t1 = (v4u){0x3f803f80u, 0x3f803f80u, 0x3f803f80u, 0};
    } else if (w == 1) {
      const unsigned j1 = (c & 2) ? 4 : (c >> 2), j2 = (c & 7) >> 1;
      t0 = (v4u){j1 == 0 ? one : 0, j1 == 1 ? one : 0, j1 == 2 ? one : 0, j1 == 3 ? one : 0};
      t1 = (v4u){j2 == 0 ? one : 0, j2 == 1 ? one : 0, j2 == 2 ? one : 0, j2 == 3 ? one : 0};
    } else {
      t0 = (v4u){16448 + g * 1728 + c * 4, 16448 + (c >> 2) * 1728 + (c & 1) * 288 + g * 16,
                 (c >> 3) * 4160 + (16 * g + 8 * ((c >> 2) & 1) + (c & 3)) * 16,
                 (c >> 3) * 262144 + (g >> 1) * 32768 + (g & 1) * 2048 + (c & 7) * 8};
      t1 = (v4u){(g & 1) * 131072 + (((g >> 1) ^ (h >> 4)) & 1) * 16384 + c * 16, lane < 16 ? lane * 8 : 0x40000000u,
                 near(4 * L + (c >> 2), 0) + 16 * g + 4 * (c & 3), g * 16384 + (c ^ (h & 15)) * 16};
    }
  }
  const unsigned rn = (r + 1) & 3, blk = 8 * (h & 3) + (h >> 2);
  const unsigned flc = WS_FLG + near(4 * L + r, 4);
  const unsigned tpb = WS_TP + r * 2 * MB + (w & 1) * 262144 + (w >> 1) * MB;
  const bool pre = L == 0 && (w == 1 || w == 2);
  const size_t rsb = pre ? (size_t)xin : (size_t)(ws + (L ? WS_X3 : WS_X1));
  unsigned par = 0;
  PAR(2, (unsigned)(size_t)out);
  PAR(3, (unsigned)((size_t)out >> 32));
  PAR(4, (h == 0 && w == 0) ? WS_END : 0u);
  PAR(5, L);
  PAR(7, r);
  PAR(8, WS_H + r * MB + L * 8192 + w * 2048);
  PAR(9, WS_IMG + r * 131072 + near(x, 0));
  PAR(10, WS_IMG + rn * 131072 + (r == 3 ? 65536 : 0) + (blk >> 4) * 32768 + (blk & 15) * 256 + w * 64);
  PAR(11, WS_MP + r * 524288 + near(4 * L + w, cm) + h * 64);
  PAR(12, WS_MP + r * 524288 + (h & 7) * 65536 + (w >> 1) * 32768 + near(x, cm) + (w & 1) * 2048);
  PAR(13, WS_HU + near(4 * L + r, 5) + h * 128);
  PAR(14, WS_HU + near(4 * L + rn, 5));
  PAR(15, flc + h * 128);
  PAR(16, flc + w * 1024);
  PAR(17, tpb + (h >> 4) * 524288 + (near(4 * L, (w >> 1)) & 0x3000) + (h & 15) * 256);
  PAR(18, tpb + (h & 7) * 32768 + near(x, (w >> 1)));
  PAR(19, (r * 32 + h) * 1024 + (pre ? (w - 1) * 131072 : w == 0 ? -262144u : 2 * MB));
  PAR(20, WS_H + r * MB + !pre * 8192 + h * 256 + (pre ? (w - 1) * 65536 : -131072u));
  PAR(22, WS_DBG + (((0 ? L : L * 4 + r) * 32 + h) * 4 + w) * 4096);
  PAR(23, ((h >> 2) & 1) * 32768 + (h & 3) * 1024 + w * 256);
  PAR(24, WS_HU + 32768 + near(4 + r, 5) + (L ? h * 128 : 0));
  PAR(25, h & 3);
  PAR(26, pre ? WS_X1 : WS_X3);
  PAR(27, WS_Y - h * 1016);
  PAR(34, (unsigned)rsb);
  PAR(35, (unsigned)(rsb >> 32));
  const unsigned lb = 1u << (lane & 31);
  const unsigned sel = lane >= 32 ? 0 : (lb & 0x1800u) ? 0x6000u : (lb & 0x101e200u) ? 0x6004u : (lb & 0x100100u) ? 0x2008u + 4 * (r >> 1)
                       : (lb & 0x60000u) ? (lane == 17 ? 0x2010u : 0x6010u) + 4 * r : 0;
  char* pt = ws + WS_PAR + blockIdx.x * 4096;
  *(uint2*)(pt + q * 8) = uint2{par, sel};
  if (w == 2) *(v4u*)(pt + 2048 + lane * 16) = t1;
  if (blockIdx.x == 1 && w < 3) {
    *(v4u*)(ws + WS_CST + w * 2048 + lane * 16) = t0;
    *(v4u*)(ws + WS_CST + w * 2048 + 1024 + lane * 16) = t1;
  }
}

__global__ void __launch_bounds__(512) k_pre(const u16* __restrict__ kin, const u16* __restrict__ vin,
                                             const u16* __restrict__ shk, const u16* __restrict__ mem0,
                                             char* __restrict__ ws, const u16* xin, u16* out,
                                             const char* __restrict__ wo, const char* __restrict__ ssk) {
  __shared__ unsigned sink[64];
  const int tid = threadIdx.x, b = blockIdx.x, x = b & 7, y = b >> 3;
  if (tid >= 256) {
    if (3 & 4) touch<1>(ws + WS_KV, 0, 0, (unsigned)(size_t)sink);
    params(ws, xin, out, tid - 256);
  }
  if (tid >= 256 && x == 0) {
    if (tid >= 260) return;
    char* p = ws + ((size_t)(y >> 2) << 21) + 65536 + (y & 3) * 8192 + (tid & 3) * 1024;
    unsigned t = 65535u;
#pragma unroll
    for (int rep = 0; rep < 3; ++rep) {
      if (rep == 1) __builtin_amdgcn_s_barrier();
      unsigned long long c0, c1;
      asm volatile("s_memtime %0\n\ts_waitcnt lgkmcnt(0)\n\tglobal_store_dword %2, %3, off sc1\n\ts_waitcnt vmcnt(0)\n\ts_memtime %1\n\ts_waitcnt lgkmcnt(0)"
                   : "=&s"(c0), "=&s"(c1) : "v"(p), "v"(~0u) : "memory");
      t = min(t, (unsigned)(c1 - c0));
    }
    *(unsigned*)(ws + WS_CTL + y * 4) = t;
    return;
  }
  if (tid >= 256) {
    const int i = y * 7 + x - 1, q = tid - 256;
    v4u im = {~0u, ~0u, ~0u, ~0u};
    if (i < 16) {
      const int c = q & 15, g = (q >> 4) & 3, k2 = (q >> 6) + (i >> 3) * 4;
      const int mrow = 2 * (c >> 2) + (c & 1), hf = (c >> 1) & 1;
      const unsigned* mp = (const unsigned*)(mem0 + mrow * 512 + 64 * k2 + 16 * g + 2 * hf);
      im = (v4u){mp[0], mp[2], mp[4], mp[6]};
    }
    if (i < 192) st16(ws + WS_H + (i / 48) * MB + (i % 48) * 4096 + q * 16, (v4u){~0u, ~0u, ~0u, ~0u});
    if (i >= 128 && i < 160) im = (v4u){0, 0, 0, 0};
    st16(ws + (i < 144 ? WS_IMG : i < 160 ? WS_HU - 144 * 4096 : WS_Y - 160 * 4096) + i * 4096 + q * 16, im);
    if (3 & 2) touch_code((y & 3) * 16384 + q * 64, (unsigned)(size_t)sink);
    if (3 == 4 && x >= 4) touch<1>(xin, ((x & 3) * 32 + y) * 16384 + q * 64, 0, (unsigned)(size_t)sink);
    if ((0 & 1) && x >= 4) touch<2>(wo, (x & 3) * MB + (y * 256 + q) * 64, MB / 2, (unsigned)(size_t)sink);
    if ((0 & 16) && x >= 4) touch<1>(ssk, (x & 3) * (MB / 2) + (y * 256 + q) * 64, 0, (unsigned)(size_t)sink);
    asm volatile("s_waitcnt vmcnt(0)");
    return;
  }
  const int ch0 = (b >> 5) * 64 + ((0 & 1) ? tid >> 3 : tid & 31) * 2, t0 = (b & 31) * 64 + ((0 & 1) ? tid & 7 : tid >> 5) * 8;
  unsigned kk[11], vv[8];
#pragma unroll
  for (int i = 0; i < 11; ++i) {
    int t = t0 - 3 + i;
    kk[i] = t >= 0 ? *(const unsigned*)(kin + t * 512 + ch0) : 0u;
  }
#pragma unroll
  for (int i = 0; i < 8; ++i) vv[i] = *(const unsigned*)(vin + (t0 + i) * 512 + ch0);
  const v4u sh = *(const v4u*)(shk + ch0 * 4);
  if (x == 0) asm volatile("s_waitcnt vmcnt(0)\n\ts_barrier" : : : "memory");
#pragma unroll
  for (int c = 0; c < 2; ++c) {
    const unsigned s0 = c ? sh.z : sh.x, s1 = c ? sh.w : sh.y;
    const float sk[4] = {bf2f(s0 & 0xffffu), bf2f(s0 >> 16), bf2f(s1 & 0xffffu), bf2f(s1 >> 16)};
    unsigned o[8];
#pragma unroll
    for (int i = 0; i < 8; ++i) {
      float a = 0.f;
#pragma unroll
      for (int t = 0; t < 4; ++t) {
        float x = bf2f(c ? kk[i + t] >> 16 : kk[i + t] & 0xffffu);
        a = rbf(a + rbf(x * sk[3 - t]));
      }
      o[i] = cbf(a * bf2f(c ? vv[i] >> 16 : vv[i] & 0xffffu));
    }
    v4u r = {o[7] | o[6] << 16, o[5] | o[4] << 16, o[3] | o[2] << 16, o[1] | o[0] << 16};
    if (0 & 2) st16(ws + WS_KV + (ch0 + c) * 4096 + (2040 - t0) * 2, r);
    else *(v4u*)(ws + WS_KV + (ch0 + c) * 4096 + (2040 - t0) * 2) = r;
  }
}

#define FSTR 4160
#define CWAVE 38480
#define CSTG 153920
__device__ __forceinline__ void conv_copies(char* sF, int q, v4u lo, v4u hi) {
  unsigned d[8] = {lo.x, lo.y, lo.z, lo.w, hi.x, hi.y, hi.z, hi.w};
#pragma unroll
  for (int s = 0; s < 8; ++s) {
    const int o = s + 1, p = o >> 1;
    v4u r;
    if (o & 1) {
      r.x = __builtin_amdgcn_alignbyte(d[p + 1], d[p], 2);
      r.y = __builtin_amdgcn_alignbyte(d[p + 2], d[p + 1], 2);
      r.z = __builtin_amdgcn_alignbyte(d[p + 3], d[p + 2], 2);
      r.w = __builtin_amdgcn_alignbyte(d[p + 4], d[p + 3], 2);
    } else {
      r.x = d[p]; r.y = d[p + 1]; r.z = d[p + 2]; r.w = d[p + 3];
    }
    *(v4u*)(sF + s * FSTR + q * 16) = r;
  }
}
__device__ __forceinline__ void conv_put(char* stg, const char* sR, const v16f& acc, float dd, int tile) {
  const int tid = threadIdx.x, w = tid >> 6, m = tid & 31, kg = (tid >> 5) & 1;
  uint2 kv[4];
#pragma unroll
  for (int r4 = 0; r4 < 4; ++r4) kv[r4] = *(const uint2*)(sR + (63 - 32 * tile - m) * 80 + (28 - 8 * r4 - 4 * kg) * 2);
#pragma unroll
  for (int r = 0; r < 16; ++r) {
    const unsigned x = (r & 2) ? kv[r >> 2].x : kv[r >> 2].y;
    const float y = rbf(acc[r]) + rbf(bf2f((r & 1) ? x & 0xffffu : x >> 16) * dd);
    *(u16*)(stg + m * 272 + (8 * (r >> 2) + 4 * kg + (r & 3)) * 8 + w * 2) = (u16)cbf(y);
  }
}
__device__ __forceinline__ void conv_send(const char* stg, int n, char* yout) {
  v4u v = *(const v4u*)(stg + (n & 31) * 272 + (threadIdx.x & 15) * 16);
  v.x = min(v.x, 0xfffffffeu); v.y = min(v.y, 0xfffffffeu); v.z = min(v.z, 0xfffffffeu); v.w = min(v.w, 0xfffffffeu);
  st16(yout + n * 32768 + (threadIdx.x & 15) * 16, v);
}
#define MF(a, b, c) c = __builtin_amdgcn_mfma_f32_32x32x16_bf16(a, b, c, 0, 0, 0)
struct ConvIn { v4u lo[5], hi[5], kv[4]; };
#define LD16(d, o, p) asm volatile("global_load_dwordx4 %0, %1, %2" : "=v"(d) : "v"(o), "s"(p) : "memory")
__device__ __forceinline__ void conv_load(ConvIn& in, int j, const u16* __restrict__ ssk, char* ws) {
  const int l = threadIdx.x & 63, ch = 4 * j + (threadIdx.x >> 6);
  const char* kv = ws + WS_KV;
  LD16(in.lo[0], ch * 4096 + max(l - 4, 0) * 16, ssk);
  LD16(in.hi[0], ch * 4096 + max(l - 3, 0) * 16, ssk);
#pragma unroll
  for (int u = 0; u < 4; ++u) LD16(in.kv[u], ch * 4096 + (3 - u) * 1024 + l * 16, kv);
#pragma unroll
  for (int u = 0; u < 4; ++u)
    st16(ws + WS_Y + j * 256 + min(8 + u * 16 + (threadIdx.x >> 4), 63u) * 32768 + (threadIdx.x & 15) * 16, (v4u){~0u, ~0u, ~0u, ~0u});
#pragma unroll
  for (int i = 1; i < 5; ++i) {
    LD16(in.lo[i], ch * 4096 + min(l + 64 * i - 4, 255) * 16, ssk);
    LD16(in.hi[i], ch * 4096 + min(l + 64 * i - 3, 255) * 16, ssk);
  }
}
__device__ __forceinline__ void conv4(char* lds, int j, ConvIn& in, unsigned dsv, char* ws, const u16* xin, const u16* wq, const u16* wv, const u16* wk, unsigned po) {
  const int tid = threadIdx.x, w = tid >> 6, l = tid & 63, m = l & 31, kg = l >> 5;
  char* sF = lds + w * CWAVE;
  char* sR = sF + 8 * FSTR;
  const v4u z = {0, 0, 0, 0};
  v4u *lo = in.lo, *hi = in.hi, *kv = in.kv;
  asm volatile("s_waitcnt vmcnt(12)" : "+v"(lo[0]), "+v"(hi[0]), "+v"(kv[0]), "+v"(kv[1]), "+v"(kv[2]), "+v"(kv[3]));
  *(v4u*)(sR + 64 * 80 + (l & 3) * 16) = z;
  conv_copies(sF, l, l < 4 ? z : lo[0], l < 3 ? z : hi[0]);
#pragma unroll
  for (int u = 0; u < 4; ++u) *(v4u*)(sR + ((3 - u) * 16 + (l >> 2)) * 80 + (l & 3) * 16) = kv[u];
  const float dd = bf2f(dsv);
  const char* fa = sF + (m & 7) * FSTR + ((m >> 3) + kg) * 16;
  const char* rb = sR + kg * 16;
  char* yout = ws + WS_Y + j * 256;
  v16f acc0 = {0}, acc1 = {0}, acc2 = {0};
  {
    v8b a[16], b[16];
#pragma unroll
    for (int e = 0; e < 16; ++e) {
      a[e] = *(const v8b*)(fa + e * 32);
      b[e] = *(const v8b*)(rb + min(63 - m + (e >> 1), 64) * 80 + (e & 1) * 32);
    }
#pragma unroll
    for (int e = 0; e < 16; ++e) MF(a[e], b[e], acc0);
    __builtin_amdgcn_sched_group_barrier(0x100, 8, 0);
#pragma unroll
    for (int e = 0; e < 16; ++e) {
      __builtin_amdgcn_sched_group_barrier(0x008, 1, 0);
      __builtin_amdgcn_sched_group_barrier(0x100, 2, 0);
    }
  }
  __builtin_amdgcn_sched_barrier(0);
  if (m < 8) conv_put(lds + CSTG, sR, acc0, dd, 0);
  asm volatile("s_waitcnt vmcnt(0)" : "+v"(lo[1]), "+v"(hi[1]), "+v"(lo[2]), "+v"(hi[2]), "+v"(lo[3]), "+v"(hi[3]), "+v"(lo[4]), "+v"(hi[4]) : : "memory");
  __syncthreads();
  if (tid < 128) conv_send(lds + CSTG, tid >> 4, yout);
  touch<2>(wv, po, 2 * MB, 162816);
#pragma unroll
  for (int i = 1; i < 4; ++i) conv_copies(sF, l + 64 * i, lo[i], hi[i]);
  if (l < 4) conv_copies(sF, l + 256, lo[4], l < 3 ? hi[4] : z);
  __builtin_amdgcn_sched_barrier(0);
  v8b a0[2][2], a1[2][2], b0[2][2], b1[2][2];
#define CLOAD(e)                                                                     \
  _Pragma("unroll") for (int u = 0; u < 2; ++u) {                                    \
    a0[(e) & 1][u] = *(const v8b*)(fa + (e) * 64 + u * 32);                          \
    b1[(e) & 1][u] = *(const v8b*)(rb + (31 - m + (e)) * 80 + u * 32);               \
    a1[(e) & 1][u] = *(const v8b*)(fa + 2048 + (e) * 64 + u * 32);                   \
    b0[(e) & 1][u] = *(const v8b*)(rb + min(63 - m + (e), 64) * 80 + u * 32);        \
  }
  touch<2>(wk, po, 2 * MB, 162816);
  touch<1>(xin, j * 16384 + threadIdx.x * 64, 0, 162816);
  CLOAD(0)
  __builtin_amdgcn_sched_barrier(0);
#pragma unroll
  for (int e = 0; e < 32; ++e) {
    if (e < 31) { CLOAD(e + 1) }
#pragma unroll
    for (int u = 0; u < 2; ++u) {
      MF(a0[e & 1][u], b1[e & 1][u], acc1);
      MF(a1[e & 1][u], b0[e & 1][u], acc2);
      if (e >= 8) MF(a0[e & 1][u], b0[e & 1][u], acc0);
    }
#pragma unroll
    for (int u = 0; u < 4; ++u) {
      __builtin_amdgcn_sched_group_barrier(0x008, 1, 0);
      __builtin_amdgcn_sched_group_barrier(0x002, 2, 0);
      __builtin_amdgcn_sched_group_barrier(0x100, 2, 0);
    }
    __builtin_amdgcn_sched_barrier(0);
  }
  __syncthreads();
  conv_put(lds + CSTG, sR, acc0, dd, 0);
  acc1 += acc2;
  conv_put(lds, sR, acc1, dd, 1);
  __syncthreads();
  conv_send(lds + CSTG, 8 + (tid >> 4), yout);
  if (tid < 128) conv_send(lds + CSTG, 24 + (tid >> 4), yout);
  conv_send(lds, 32 + (tid >> 4), yout);
  conv_send(lds, 48 + (tid >> 4), yout);
  __syncthreads();
}

__device__ __forceinline__ void fetch_wo(const u16* wo, unsigned h, unsigned w, unsigned lane) {
  const v4u srd = {(unsigned)(size_t)wo, (unsigned)((size_t)wo >> 32) & 0xffffu, 4 * MB, 0x20000u};
  const unsigned v0 = (lane * 16) ^ ((w & 1) * 128);
  unsigned t;
  asm volatile(
    "s_nop 4\n"
    "s_add_u32 m0, %2, 0\n"
    "s_add_u32 %0, %3, 0\n"
    "buffer_load_dwordx4 %4, %1, %0 offen lds\n"
    "s_add_u32 m0, %2, 1024\n"
    "s_add_u32 %0, %3, 16384\n"
    "buffer_load_dwordx4 %4, %1, %0 offen lds\n"
    "s_add_u32 m0, %2, 2048\n"
    "s_add_u32 %0, %3, 1024\n"
    "buffer_load_dwordx4 %4, %1, %0 offen lds\n"
    "s_add_u32 m0, %2, 3072\n"
    "s_add_u32 %0, %3, 17408\n"
    "buffer_load_dwordx4 %4, %1, %0 offen lds\n"
    "s_add_u32 m0, %2, 16384\n"
    "s_add_u32 %0, %3, 2048\n"
    "buffer_load_dwordx4 %5, %1, %0 offen lds\n"
    "s_add_u32 m0, %2, 17408\n"
    "s_add_u32 %0, %3, 18432\n"
    "buffer_load_dwordx4 %5, %1, %0 offen lds\n"
    "s_add_u32 m0, %2, 18432\n"
    "s_add_u32 %0, %3, 3072\n"
    "buffer_load_dwordx4 %5, %1, %0 offen lds\n"
    "s_add_u32 m0, %2, 19456\n"
    "s_add_u32 %0, %3, 19456\n"
    "buffer_load_dwordx4 %5, %1, %0 offen lds\n"
    : "=&s"(t)
    : "s"(srd), "s"(32768 + w * 32768), "s"(h * 131072 + w * 4096), "v"(v0), "v"(v0 ^ 64)
    : "scc", "memory");
}
__global__ void __launch_bounds__(256, 1) k_main(char* __restrict__ ws, const u16* __restrict__ xin,
                                                 const u16* __restrict__ ssk, const u16* __restrict__ ssd, const u16* __restrict__ wq,
                                                 const u16* __restrict__ wk, const u16* __restrict__ wv, const u16* __restrict__ wo) {
  __shared__ __attribute__((aligned(16))) char lds[163840];
  const unsigned tid = threadIdx.x, lane = tid & 63;
  const unsigned w = __builtin_amdgcn_readfirstlane(tid >> 6);
  const unsigned x = blockIdx.x & 7, y = blockIdx.x >> 3;
  const unsigned L = x >> 2;
  const unsigned r = y & 3, h = (x & 3) * 8 + (y >> 2);
  if (3 & 1) touch_code(r * 16384 + tid * 64, L ? 162816 : 2048);
  asm volatile("" : : "s"(ws), "s"(xin), "s"(ssk), "s"(ssd), "s"(wq), "s"(wk), "s"(wv), "s"(wo));
  if (!L) {
    const unsigned p = (tid >> 1) * 8192 + h * 256 + (tid & 1) * 128, pp = h * 131072 + tid * 128;
    if (r == 1) {
      touch<1>(wo + 16384, pp, 0, 2048);
      touch<1>(wq + 0, p, 0, 2048);
    }
    if (r == 2) {
      touch<1>(wo + 32768, pp, 0, 2048);
      touch<1>(wq + 524288, p, 0, 2048);
      touch<1>(wq + 1572864, p, 0, 2048);
      touch<1>(wv + 524288, p, 0, 2048);
      touch<1>(wv + 1572864, p, 0, 2048);
    }
    if (r == 3) {
      touch<1>(wo + 49152, pp, 0, 2048);
      touch<1>(wq + 1048576, p, 0, 2048);
      touch<1>(wv + 0, p, 0, 2048);
      touch<1>(wv + 1048576, p, 0, 2048);
      touch<1>(wk + 0, p, 0, 2048);
      touch<1>(wk + 524288, p, 0, 2048);
      touch<1>(wk + 1048576, p, 0, 2048);
    }
    fetch_wo(wo, h, w, lane);
  }
  ConvIn in;
  const unsigned po = (y * 8 + (tid >> 5)) * 8192 + (x & 3) * 2048 + (tid & 31) * 64;
  if (L) {
    unsigned dsv;
    asm volatile("s_load_dword %0, %1, %2" : "=&s"(dsv) : "s"(ssd), "s"(((x & 3) * 64 + y * 2 + (w >> 1)) * 4));
    const int j = (x & 3) * 32 + y;
    conv_load(in, j, ssk, ws);
    if (0) touch<2>(wo, (x & 3) * MB + (y >> 2) * 131072 + r * 16384 + tid * 64, 65536, 162816);
    asm volatile("s_waitcnt lgkmcnt(0)" : "+s"(dsv));
    conv4(lds, j, in, (dsv >> (16 * (w & 1))) & 0xffffu, ws, xin, wq, wv, wk, po);
    fetch_wo(wo, h, w, lane);
  }
  asm volatile(
    "v_mbcnt_lo_u32_b32 v0, -1, 0\n"
    "v_mbcnt_hi_u32_b32 v0, -1, v0\n"
    "v_lshlrev_b32 v0, 4, v0\n"
    "s_waitcnt lgkmcnt(0)\n"
    "s_and_b32 s9, s9, 0xffff\n"
    "s_mov_b32 s10, 0x3400000\n"
    "s_mov_b32 s11, 0x20000\n"
    "s_lshl_b32 s66, s41, 15\n"
    "s_add_u32 s66, s66, 0x8000\n"
    "s_lshl_b32 s67, s41, 10\n"
    "s_lshl_b32 s68, s41, 6\n"
    "s_mov_b32 s82, 0x400000\n"
    "s_mov_b32 s83, 0x20000\n"
    "s_mov_b32 s80, s78\n"
    "s_and_b32 s81, s79, 0xffff\n"
    "s_and_b32 s63, s59, 0x1f00\n"
    "s_lshl_b32 s63, s63, 7\n"
    "s_add_u32 s63, s63, s67\n"
    "s_lshl_b32 s63, s63, 2\n"
    "s_and_b32 s61, s41, 1\n"
    "s_lshl_b32 s61, s61, 7\n"
    "v_xor_b32 v8, s61, v0\n"
    "v_xor_b32 v9, 64, v8\n"
    "s_lshl_b32 s62, s60, 12\n"
    "s_add_u32 s62, s62, 0x1a10000\n"
    "s_lshr_b32 s61, s67, 1\n"
    "s_add_u32 s61, s61, s62\n"
    "v_lshrrev_b32 v2, 1, v0\n"
    "buffer_load_dwordx2 v[2:3], v2, s[8:11], s61 offen\n"
    "s_mov_b32 s61, 0x1a00000\n"
    "v_lshrrev_b32 v1, 2, v0\n"
    "buffer_load_dword v1, v1, s[8:11], s61 offen\n"
    "buffer_load_dwordx4 v[124:127], v0, s[8:11], s62 offen offset:2048\n"
    "s_add_u32 m0, s67, 0x6040\n"
    "s_add_u32 s61, s67, 0x1a08000\n"
    "buffer_load_dwordx4 v0, s[8:11], s61 offen lds\n"
    "s_mov_b32 m0, 0x7040\n"
    "s_mov_b32 s61, 0x1a09000\n"
    "buffer_load_dwordx4 v0, s[8:11], s61 offen lds\n"
    "s_add_u32 m0, s66, 0x1000\n"
    "s_add_u32 s61, s63, 0x8000\n"
    "buffer_load_dwordx4 v8, s[80:83], s61 offen lds\n"
    "s_add_u32 m0, s66, 0x1400\n"
    "s_add_u32 s61, s63, 0xc000\n"
    "buffer_load_dwordx4 v8, s[80:83], s61 offen lds\n"
    "s_add_u32 m0, s66, 0x1800\n"
    "s_add_u32 s61, s63, 0x8400\n"
    "buffer_load_dwordx4 v8, s[80:83], s61 offen lds\n"
    "s_add_u32 m0, s66, 0x1c00\n"
    "s_add_u32 s61, s63, 0xc400\n"
    "buffer_load_dwordx4 v8, s[80:83], s61 offen lds\n"
    "s_add_u32 m0, s66, 0x5000\n"
    "s_add_u32 s61, s63, 0x8800\n"
    "buffer_load_dwordx4 v9, s[80:83], s61 offen lds\n"
    "s_add_u32 m0, s66, 0x5400\n"
    "s_add_u32 s61, s63, 0xc800\n"
    "buffer_load_dwordx4 v9, s[80:83], s61 offen lds\n"
    "s_add_u32 m0, s66, 0x5800\n"
    "s_add_u32 s61, s63, 0x8c00\n"
    "buffer_load_dwordx4 v9, s[80:83], s61 offen lds\n"
    "s_add_u32 m0, s66, 0x5c00\n"
    "s_add_u32 s61, s63, 0xcc00\n"
    "buffer_load_dwordx4 v9, s[80:83], s61 offen lds\n"
    "s_waitcnt vmcnt(11)\n"
    "v_min_u32_dpp v4, v1, v1 quad_perm:[2,3,0,1] row_mask:0xf bank_mask:0xf\n"
    "v_min_u32_dpp v6, v1, v1 quad_perm:[1,0,3,2] row_mask:0xf bank_mask:0xf\n"
    "s_nop 0\n"
    "v_mov_b32_dpp v5, v4 quad_perm:[1,0,3,2] row_mask:0xf bank_mask:0xf\n"
    "v_mov_b32_dpp v7, v6 quad_perm:[2,3,0,1] row_mask:0xf bank_mask:0xf\n"
    "v_cmp_lt_u32 vcc, v7, v6\n"
    "s_and_b32 s62, vcc_lo, 0x11111111\n"
    "s_bcnt1_i32_b32 s62, s62\n"
    "s_cmp_gt_u32 s62, 4\n"
    "s_cselect_b32 s62, 0x4000, 0\n"
    "v_cmp_lt_u32 vcc, v5, v4\n"
    "s_nop 1\n"
    "v_mov_b32 v4, vcc_lo\n"
    "s_lshl_b32 s61, vcc_lo, 9\n"
    "v_lshrrev_b32 v4, v3, v4\n"
    "v_and_b32 v4, 1, v4\n"
    "v_lshl_or_b32 v4, v4, 13, s62\n"
    "v_and_b32 v4, v4, v3\n"
    "v_xor_b32 v2, v2, v4\n"
    "s_and_b32 s61, s61, 0x2000\n"
    "s_or_b32 s61, s61, s62\n"
    "s_waitcnt vmcnt(10)\n"
    "v_xor_b32 v124, s62, v124\n"
    "v_xor_b32 v126, s61, v126\n"
    "ds_write_b128 v0, v[124:127] offset:29760\n"
    "v_readlane_b32 s40, v2, 5\n"
    "v_readlane_b32 s42, v2, 7\n"
    "v_readlane_b32 s45, v2, 8\n"
    "v_readlane_b32 s46, v2, 9\n"
    "v_readlane_b32 s47, v2, 10\n"
    "v_readlane_b32 s48, v2, 11\n"
    "v_readlane_b32 s50, v2, 12\n"
    "v_readlane_b32 s51, v2, 13\n"
    "v_readlane_b32 s52, v2, 14\n"
    "v_readlane_b32 s53, v2, 15\n"
    "v_readlane_b32 s54, v2, 16\n"
    "v_readlane_b32 s55, v2, 17\n"
    "v_readlane_b32 s56, v2, 18\n"
    "v_readlane_b32 s57, v2, 19\n"
    "v_readlane_b32 s58, v2, 20\n"
    "v_readlane_b32 s60, v2, 22\n"
    "v_readlane_b32 s49, v2, 23\n"
    "v_readlane_b32 s70, v2, 24\n"
    "v_readlane_b32 s61, v2, 25\n"
    "v_readlane_b32 s36, v2, 26\n"
    "v_readlane_b32 s3, v2, 27\n"
    "s_mov_b64 s[12:13], s[8:9]\n"
    "s_mov_b32 s14, 0\n"
    "s_mov_b32 s15, 0x20000\n"
    "s_mov_b64 s[16:17], s[8:9]\n"
    "v_readlane_b32 s18, v2, 4\n"
    "s_mov_b32 s19, 0x20000\n"
    "v_readlane_b32 s20, v2, 2\n"
    "v_readlane_b32 s21, v2, 3\n"
    "s_and_b32 s21, s21, 0xffff\n"
    "s_mov_b32 s22, 0x200000\n"
    "s_mov_b32 s23, 0x20000\n"
    "v_readlane_b32 s4, v2, 34\n"
    "v_readlane_b32 s5, v2, 35\n"
    "s_and_b32 s5, s5, 0xffff\n"
    "s_mov_b32 s6, 0\n"
    "s_mov_b32 s7, 0x20000\n"
    "s_getpc_b64 s[38:39]\n"
    "anchor_%=:\n"
    "s_mov_b32 s62, tail0_%=-anchor_%=\n"
    "s_cmp_eq_u32 s61, 1\n"
    "s_cselect_b32 s62, tail1_%=-anchor_%=, s62\n"
    "s_cmp_eq_u32 s61, 2\n"
    "s_cselect_b32 s62, tail2_%=-anchor_%=, s62\n"
    "s_cmp_eq_u32 s61, 3\n"
    "s_cselect_b32 s62, tail3_%=-anchor_%=, s62\n"
    "s_cmp_eq_u32 s61, 0\n"
    "s_cselect_b32 s2, 0x340003e, 0\n"
    "s_add_u32 s38, s38, s62\n"
    "s_addc_u32 s39, s39, 0\n"
    "s_cmp_eq_u32 s61, 0\n"
    "s_cselect_b32 s62, -16, -384\n"
    "s_lshl2_add_u32 s24, s61, s41\n"
    "s_cmp_eq_u32 s24, 1\n"
    "s_cselect_b32 s62, padslowI0_%=-tail0_%=, s62\n"
    "s_cmp_eq_u32 s24, 2\n"
    "s_cselect_b32 s62, padslowR00_%=-tail0_%=, s62\n"
    "s_cmp_eq_u32 s24, 3\n"
    "s_cselect_b32 s62, padhuagain0_%=-tail0_%=, s62\n"
    "s_cmp_eq_u32 s24, 5\n"
    "s_cselect_b32 s62, padslowI0_%=-tail1_%=, s62\n"
    "s_cmp_eq_u32 s24, 6\n"
    "s_cselect_b32 s62, padslowR10_%=-tail1_%=, s62\n"
    "s_cmp_eq_u32 s24, 7\n"
    "s_cselect_b32 s62, padhuagain0_%=-tail1_%=, s62\n"
    "s_cmp_eq_u32 s24, 9\n"
    "s_cselect_b32 s62, padslowI0_%=-tail2_%=, s62\n"
    "s_cmp_eq_u32 s24, 10\n"
    "s_cselect_b32 s62, padslowR20_%=-tail2_%=, s62\n"
    "s_cmp_eq_u32 s24, 11\n"
    "s_cselect_b32 s62, padhuagain0_%=-tail2_%=, s62\n"
    "s_cmp_eq_u32 s24, 13\n"
    "s_cselect_b32 s62, padslowI0_%=-tail3_%=, s62\n"
    "s_cmp_eq_u32 s24, 14\n"
    "s_cselect_b32 s62, padslowR30_%=-tail3_%=, s62\n"
    "s_cmp_eq_u32 s24, 15\n"
    "s_cselect_b32 s62, padhuagain0_%=-tail3_%=, s62\n"
    "s_ashr_i32 s25, s62, 31\n"
    "s_add_u32 s24, s38, s62\n"
    "s_addc_u32 s25, s39, s25\n"
    "s_mov_b64 exec, 0\n"
    "s_setpc_b64 s[24:25]\n"
    "dryret_%=:\n"
    "s_mov_b64 exec, -1\n"
    "s_mov_b32 s44, 0x30d40\n"
    "s_mov_b32 s0, 0\n"
    "s_mov_b32 s65, 32\n"
    "s_mov_b32 s71, 0xa8\n"
    "s_cmp_eq_u32 s2, 0\n"
    "s_cselect_b32 s37, 12, 0\n"
    "s_add_u32 s37, s37, 0x78\n"
    "s_add_u32 s34, s46, 0x8000\n"
    "s_xor_b32 s35, s56, 0x4000\n"
    "s_add_u32 s35, s35, 0x80000\n"
    "s_mov_b32 s97, 0\n"
    "s_mov_b32 s98, 0\n"
    "s_mov_b32 s99, 0\n"
    "s_mov_b32 s32, 0x3e0293ee\n"
    "v_and_b32 v3, 32, v0\n"
    "v_cmp_eq_u32 vcc, 0, v3\n"
    "v_mov_b32 v1, 0xeeee\n"
    "v_mov_b32 v2, 0x4444\n"
    "v_cndmask_b32 v1, v1, v2, vcc\n"
    "s_mov_b32 s69, 1\n"
    "s_add_u32 m0, s66, 0x2000\n"
    "s_add_u32 s61, s63, 0x10000\n"
    "buffer_load_dwordx4 v8, s[80:83], s61 offen lds\n"
    "s_add_u32 m0, s66, 0x2400\n"
    "s_add_u32 s61, s63, 0x14000\n"
    "buffer_load_dwordx4 v8, s[80:83], s61 offen lds\n"
    "s_add_u32 m0, s66, 0x2800\n"
    "s_add_u32 s61, s63, 0x10400\n"
    "buffer_load_dwordx4 v8, s[80:83], s61 offen lds\n"
    "s_add_u32 m0, s66, 0x2c00\n"
    "s_add_u32 s61, s63, 0x14400\n"
    "buffer_load_dwordx4 v8, s[80:83], s61 offen lds\n"
    "s_add_u32 m0, s66, 0x6000\n"
    "s_add_u32 s61, s63, 0x10800\n"
    "buffer_load_dwordx4 v9, s[80:83], s61 offen lds\n"
    "s_add_u32 m0, s66, 0x6400\n"
    "s_add_u32 s61, s63, 0x14800\n"
    "buffer_load_dwordx4 v9, s[80:83], s61 offen lds\n"
    "s_add_u32 m0, s66, 0x6800\n"
    "s_add_u32 s61, s63, 0x10c00\n"
    "buffer_load_dwordx4 v9, s[80:83], s61 offen lds\n"
    "s_add_u32 m0, s66, 0x6c00\n"
    "s_add_u32 s61, s63, 0x14c00\n"
    "buffer_load_dwordx4 v9, s[80:83], s61 offen lds\n"
    "s_add_u32 m0, s66, 0x3000\n"
    "s_add_u32 s61, s63, 0x18000\n"
    "buffer_load_dwordx4 v8, s[80:83], s61 offen lds\n"
    "s_add_u32 m0, s66, 0x3400\n"
    "s_add_u32 s61, s63, 0x1c000\n"
    "buffer_load_dwordx4 v8, s[80:83], s61 offen lds\n"
    "s_add_u32 m0, s66, 0x3800\n"
    "s_add_u32 s61, s63, 0x18400\n"
    "buffer_load_dwordx4 v8, s[80:83], s61 offen lds\n"
    "s_add_u32 m0, s66, 0x3c00\n"
    "s_add_u32 s61, s63, 0x1c400\n"
    "buffer_load_dwordx4 v8, s[80:83], s61 offen lds\n"
    "s_add_u32 m0, s66, 0x7000\n"
    "s_add_u32 s61, s63, 0x18800\n"
    "buffer_load_dwordx4 v9, s[80:83], s61 offen lds\n"
    "s_add_u32 m0, s66, 0x7400\n"
    "s_add_u32 s61, s63, 0x1c800\n"
    "buffer_load_dwordx4 v9, s[80:83], s61 offen lds\n"
    "s_add_u32 m0, s66, 0x7800\n"
    "s_add_u32 s61, s63, 0x18c00\n"
    "buffer_load_dwordx4 v9, s[80:83], s61 offen lds\n"
    "s_add_u32 m0, s66, 0x7c00\n"
    "s_add_u32 s61, s63, 0x1cc00\n"
    "buffer_load_dwordx4 v9, s[80:83], s61 offen lds\n"
    "v_add_u32 v66, s66, v0\n"
    "v_lshrrev_b32 v2, 8, v0\n"
    "v_lshlrev_b32 v3, 5, v2\n"
    "v_and_b32 v67, 0xf0, v0\n"
    "v_xor_b32 v67, v67, v3\n"
    "v_lshl_or_b32 v67, v2, 13, v67\n"
    "v_xor_b32 v68, 0x80, v67\n"
    "v_bfe_u32 v3, v0, 6, 3\n"
    "v_lshlrev_b32 v69, 5, v3\n"
    "v_xor_b32 v69, s68, v69\n"
    "v_bfe_u32 v3, v0, 6, 2\n"
    "v_lshl_or_b32 v69, v3, 8, v69\n"
    "v_bfe_u32 v3, v0, 8, 1\n"
    "v_lshl_or_b32 v69, v3, 11, v69\n"
    "v_bfe_u32 v3, v0, 9, 1\n"
    "v_lshl_or_b32 v69, v3, 14, v69\n"
    "v_bfe_u32 v3, v0, 4, 2\n"
    "v_lshl_or_b32 v69, v3, 3, v69\n"
    "v_add_u32 v69, 0x8000, v69\n"
    "v_xor_b32 v70, 32, v69\n"
    "v_add_u32 v71, 0x10000, v69\n"
    "v_add_u32 v72, 0x10000, v70\n"
    "v_lshlrev_b32 v73, 6, v2\n"
    "v_xor_b32 v73, s68, v73\n"
    "v_lshl_or_b32 v73, v2, 14, v73\n"
    "v_bfe_u32 v3, v0, 2, 6\n"
    "v_add_u32 v73, v73, v3\n"
    "v_add_u32 v73, 0x8000, v73\n"
    "v_add_u32 v74, 0x10000, v73\n"
    "s_mov_b32 s64, 0x5040100\n"
    "s_mov_b32 s69, 0x7060302\n"
    "s_waitcnt vmcnt(29)\n"
    "s_waitcnt lgkmcnt(0)\n"
    "s_barrier\n"
    "s_cmp_lg_u32 s69, 0\n"
    "s_cselect_b32 s62, s57, -1\n"
    "s_cmp_lt_u32 s62, 0x200000\n"
    "s_cselect_b32 s6, 0x200000, 0\n"
    "s_cselect_b32 s14, 0x3400000, 0\n"
    "s_cselect_b32 s62, s57, 0\n"
    "s_cmp_eq_u32 s40, 0\n"
    "s_cselect_b32 s14, s14, 0\n"
    "buffer_load_dwordx4 v[116:119], v0, s[4:7], s62 offen sc1\n"
    "ds_read2st64_b32 v[6:7], v73 offset0:0 offset1:4\n"
    "ds_read2st64_b32 v[8:9], v73 offset0:8 offset1:12\n"
    "ds_read2st64_b32 v[10:11], v74 offset0:0 offset1:4\n"
    "ds_read2st64_b32 v[12:13], v74 offset0:8 offset1:12\n"
    "ds_read2st64_b32 v[14:15], v73 offset0:1 offset1:5\n"
    "ds_read2st64_b32 v[16:17], v73 offset0:9 offset1:13\n"
    "ds_read2st64_b32 v[18:19], v74 offset0:1 offset1:5\n"
    "ds_read2st64_b32 v[20:21], v74 offset0:9 offset1:13\n"
    "ds_read2st64_b32 v[22:23], v73 offset0:2 offset1:6\n"
    "ds_read2st64_b32 v[24:25], v73 offset0:10 offset1:14\n"
    "ds_read2st64_b32 v[26:27], v74 offset0:2 offset1:6\n"
    "ds_read2st64_b32 v[28:29], v74 offset0:10 offset1:14\n"
    "ds_read2st64_b32 v[30:31], v73 offset0:3 offset1:7\n"
    "ds_read2st64_b32 v[32:33], v73 offset0:11 offset1:15\n"
    "ds_read2st64_b32 v[34:35], v74 offset0:3 offset1:7\n"
    "ds_read2st64_b32 v[50:51], v74 offset0:11 offset1:15\n"
    "s_waitcnt vmcnt(17)\n"
    "s_waitcnt lgkmcnt(0)\n"
    "s_barrier\n"
    "s_mov_b32 s80, s72\n"
    "s_and_b32 s81, s73, 0xffff\n"
    "s_add_u32 m0, s66, 0\n"
    "s_add_u32 s61, s59, 0\n"
    "buffer_load_dwordx4 v67, s[80:83], s61 offen lds\n"
    "ds_read2st64_b32 v[2:3], v73 offset0:16 offset1:20\n"
    "ds_read2st64_b32 v[4:5], v73 offset0:24 offset1:28\n"
    "v_perm_b32 v128, v7, v6, s64\n"
    "v_perm_b32 v129, v7, v6, s69\n"
    "v_perm_b32 v130, v9, v8, s64\n"
    "v_perm_b32 v131, v9, v8, s69\n"
    "s_add_u32 m0, s66, 0x400\n"
    "s_add_u32 s61, s59, 0x8000\n"
    "buffer_load_dwordx4 v67, s[80:83], s61 offen lds\n"
    "ds_read2st64_b32 v[6:7], v74 offset0:16 offset1:20\n"
    "ds_read2st64_b32 v[8:9], v74 offset0:24 offset1:28\n"
    "v_perm_b32 v132, v11, v10, s64\n"
    "v_perm_b32 v133, v11, v10, s69\n"
    "v_perm_b32 v134, v13, v12, s64\n"
    "v_perm_b32 v135, v13, v12, s69\n"
    "s_add_u32 m0, s66, 0x800\n"
    "s_add_u32 s61, s59, 0x10000\n"
    "buffer_load_dwordx4 v68, s[80:83], s61 offen lds\n"
    "ds_read2st64_b32 v[10:11], v73 offset0:17 offset1:21\n"
    "ds_read2st64_b32 v[12:13], v73 offset0:25 offset1:29\n"
    "v_perm_b32 v160, v15, v14, s64\n"
    "v_perm_b32 v161, v15, v14, s69\n"
    "v_perm_b32 v162, v17, v16, s64\n"
    "v_perm_b32 v163, v17, v16, s69\n"
    "s_add_u32 m0, s66, 0xc00\n"
    "s_add_u32 s61, s59, 0x18000\n"
    "buffer_load_dwordx4 v68, s[80:83], s61 offen lds\n"
    "ds_read2st64_b32 v[14:15], v74 offset0:17 offset1:21\n"
    "ds_read2st64_b32 v[16:17], v74 offset0:25 offset1:29\n"
    "v_perm_b32 v164, v19, v18, s64\n"
    "v_perm_b32 v165, v19, v18, s69\n"
    "v_perm_b32 v166, v21, v20, s64\n"
    "v_perm_b32 v167, v21, v20, s69\n"
    "s_add_u32 m0, s66, 0x4000\n"
    "s_add_u32 s61, s59, 0x20000\n"
    "buffer_load_dwordx4 v67, s[80:83], s61 offen lds\n"
    "ds_read2st64_b32 v[18:19], v73 offset0:18 offset1:22\n"
    "ds_read2st64_b32 v[20:21], v73 offset0:26 offset1:30\n"
    "v_perm_b32 v192, v23, v22, s64\n"
    "v_perm_b32 v193, v23, v22, s69\n"
    "v_perm_b32 v194, v25, v24, s64\n"
    "v_perm_b32 v195, v25, v24, s69\n"
    "s_add_u32 m0, s66, 0x4400\n"
    "s_add_u32 s61, s59, 0x28000\n"
    "buffer_load_dwordx4 v67, s[80:83], s61 offen lds\n"
    "ds_read2st64_b32 v[22:23], v74 offset0:18 offset1:22\n"
    "ds_read2st64_b32 v[24:25], v74 offset0:26 offset1:30\n"
    "v_perm_b32 v196, v27, v26, s64\n"
    "v_perm_b32 v197, v27, v26, s69\n"
    "v_perm_b32 v198, v29, v28, s64\n"
    "v_perm_b32 v199, v29, v28, s69\n"
    "s_add_u32 m0, s66, 0x4800\n"
    "s_add_u32 s61, s59, 0x30000\n"
    "buffer_load_dwordx4 v68, s[80:83], s61 offen lds\n"
    "ds_read2st64_b32 v[26:27], v73 offset0:19 offset1:23\n"
    "ds_read2st64_b32 v[28:29], v73 offset0:27 offset1:31\n"
    "v_perm_b32 v224, v31, v30, s64\n"
    "v_perm_b32 v225, v31, v30, s69\n"
    "v_perm_b32 v226, v33, v32, s64\n"
    "v_perm_b32 v227, v33, v32, s69\n"
    "s_add_u32 m0, s66, 0x4c00\n"
    "s_add_u32 s61, s59, 0x38000\n"
    "buffer_load_dwordx4 v68, s[80:83], s61 offen lds\n"
    "ds_read2st64_b32 v[30:31], v74 offset0:19 offset1:23\n"
    "ds_read2st64_b32 v[32:33], v74 offset0:27 offset1:31\n"
    "v_perm_b32 v228, v35, v34, s64\n"
    "v_perm_b32 v229, v35, v34, s69\n"
    "v_perm_b32 v230, v51, v50, s64\n"
    "v_perm_b32 v231, v51, v50, s69\n"
    "s_cmp_eq_u32 s40, 0\n"
    "s_cbranch_scc0 yt0_%=\n"
    "ytend_%=:\n"
    "s_waitcnt vmcnt(17)\n"
    "s_waitcnt lgkmcnt(0)\n"
    "s_barrier\n"
    "s_add_u32 m0, s66, 0x1000\n"
    "s_add_u32 s61, s59, 0x100000\n"
    "buffer_load_dwordx4 v67, s[80:83], s61 offen lds\n"
    "ds_read2st64_b32 v[34:35], v73 offset0:32 offset1:36\n"
    "ds_read2st64_b32 v[36:37], v73 offset0:40 offset1:44\n"
    "v_perm_b32 v136, v3, v2, s64\n"
    "v_perm_b32 v137, v3, v2, s69\n"
    "v_perm_b32 v138, v5, v4, s64\n"
    "v_perm_b32 v139, v5, v4, s69\n"
    "s_add_u32 m0, s66, 0x1400\n"
    "s_add_u32 s61, s59, 0x108000\n"
    "buffer_load_dwordx4 v67, s[80:83], s61 offen lds\n"
    "ds_read2st64_b32 v[38:39], v74 offset0:32 offset1:36\n"
    "ds_read2st64_b32 v[40:41], v74 offset0:40 offset1:44\n"
    "v_perm_b32 v140, v7, v6, s64\n"
    "v_perm_b32 v141, v7, v6, s69\n"
    "v_perm_b32 v142, v9, v8, s64\n"
    "v_perm_b32 v143, v9, v8, s69\n"
    "s_add_u32 m0, s66, 0x1800\n"
    "s_add_u32 s61, s59, 0x110000\n"
    "buffer_load_dwordx4 v68, s[80:83], s61 offen lds\n"
    "ds_read2st64_b32 v[42:43], v73 offset0:33 offset1:37\n"
    "ds_read2st64_b32 v[44:45], v73 offset0:41 offset1:45\n"
    "v_perm_b32 v168, v11, v10, s64\n"
    "v_perm_b32 v169, v11, v10, s69\n"
    "v_perm_b32 v170, v13, v12, s64\n"
    "v_perm_b32 v171, v13, v12, s69\n"
    "s_add_u32 m0, s66, 0x1c00\n"
    "s_add_u32 s61, s59, 0x118000\n"
    "buffer_load_dwordx4 v68, s[80:83], s61 offen lds\n"
    "ds_read2st64_b32 v[46:47], v74 offset0:33 offset1:37\n"
    "ds_read2st64_b32 v[48:49], v74 offset0:41 offset1:45\n"
    "v_perm_b32 v172, v15, v14, s64\n"
    "v_perm_b32 v173, v15, v14, s69\n"
    "v_perm_b32 v174, v17, v16, s64\n"
    "v_perm_b32 v175, v17, v16, s69\n"
    "s_add_u32 m0, s66, 0x5000\n"
    "s_add_u32 s61, s59, 0x120000\n"
    "buffer_load_dwordx4 v67, s[80:83], s61 offen lds\n"
    "ds_read2st64_b32 v[50:51], v73 offset0:34 offset1:38\n"
    "ds_read2st64_b32 v[52:53], v73 offset0:42 offset1:46\n"
    "v_perm_b32 v200, v19, v18, s64\n"
    "v_perm_b32 v201, v19, v18, s69\n"
    "v_perm_b32 v202, v21, v20, s64\n"
    "v_perm_b32 v203, v21, v20, s69\n"
    "s_add_u32 m0, s66, 0x5400\n"
    "s_add_u32 s61, s59, 0x128000\n"
    "buffer_load_dwordx4 v67, s[80:83], s61 offen lds\n"
    "ds_read2st64_b32 v[54:55], v74 offset0:34 offset1:38\n"
    "ds_read2st64_b32 v[56:57], v74 offset0:42 offset1:46\n"
    "v_perm_b32 v204, v23, v22, s64\n"
    "v_perm_b32 v205, v23, v22, s69\n"
    "v_perm_b32 v206, v25, v24, s64\n"
    "v_perm_b32 v207, v25, v24, s69\n"
    "s_add_u32 m0, s66, 0x5800\n"
    "s_add_u32 s61, s59, 0x130000\n"
    "buffer_load_dwordx4 v68, s[80:83], s61 offen lds\n"
    "ds_read2st64_b32 v[58:59], v73 offset0:35 offset1:39\n"
    "ds_read2st64_b32 v[60:61], v73 offset0:43 offset1:47\n"
    "v_perm_b32 v232, v27, v26, s64\n"
    "v_perm_b32 v233, v27, v26, s69\n"
    "v_perm_b32 v234, v29, v28, s64\n"
    "v_perm_b32 v235, v29, v28, s69\n"
    "s_add_u32 m0, s66, 0x5c00\n"
    "s_add_u32 s61, s59, 0x138000\n"
    "buffer_load_dwordx4 v68, s[80:83], s61 offen lds\n"
    "ds_read2st64_b32 v[62:63], v74 offset0:35 offset1:39\n"
    "ds_read2st64_b32 v[64:65], v74 offset0:43 offset1:47\n"
    "v_perm_b32 v236, v31, v30, s64\n"
    "v_perm_b32 v237, v31, v30, s69\n"
    "v_perm_b32 v238, v33, v32, s64\n"
    "v_perm_b32 v239, v33, v32, s69\n"
    "s_waitcnt vmcnt(17)\n"
    "s_waitcnt lgkmcnt(0)\n"
    "s_barrier\n"
    "s_add_u32 m0, s66, 0x2000\n"
    "s_add_u32 s61, s59, 0x200000\n"
    "buffer_load_dwordx4 v67, s[80:83], s61 offen lds\n"
    "ds_read2st64_b32 v[2:3], v73 offset0:48 offset1:52\n"
    "ds_read2st64_b32 v[4:5], v73 offset0:56 offset1:60\n"
    "v_perm_b32 v144, v35, v34, s64\n"
    "v_perm_b32 v145, v35, v34, s69\n"
    "v_perm_b32 v146, v37, v36, s64\n"
    "v_perm_b32 v147, v37, v36, s69\n"
    "s_add_u32 m0, s66, 0x2400\n"
    "s_add_u32 s61, s59, 0x208000\n"
    "buffer_load_dwordx4 v67, s[80:83], s61 offen lds\n"
    "ds_read2st64_b32 v[6:7], v74 offset0:48 offset1:52\n"
    "ds_read2st64_b32 v[8:9], v74 offset0:56 offset1:60\n"
    "v_perm_b32 v148, v39, v38, s64\n"
    "v_perm_b32 v149, v39, v38, s69\n"
    "v_perm_b32 v150, v41, v40, s64\n"
    "v_perm_b32 v151, v41, v40, s69\n"
    "s_add_u32 m0, s66, 0x2800\n"
    "s_add_u32 s61, s59, 0x210000\n"
    "buffer_load_dwordx4 v68, s[80:83], s61 offen lds\n"
    "ds_read2st64_b32 v[10:11], v73 offset0:49 offset1:53\n"
    "ds_read2st64_b32 v[12:13], v73 offset0:57 offset1:61\n"
    "v_perm_b32 v176, v43, v42, s64\n"
    "v_perm_b32 v177, v43, v42, s69\n"
    "v_perm_b32 v178, v45, v44, s64\n"
    "v_perm_b32 v179, v45, v44, s69\n"
    "s_add_u32 m0, s66, 0x2c00\n"
    "s_add_u32 s61, s59, 0x218000\n"
    "buffer_load_dwordx4 v68, s[80:83], s61 offen lds\n"
    "ds_read2st64_b32 v[14:15], v74 offset0:49 offset1:53\n"
    "ds_read2st64_b32 v[16:17], v74 offset0:57 offset1:61\n"
    "v_perm_b32 v180, v47, v46, s64\n"
    "v_perm_b32 v181, v47, v46, s69\n"
    "v_perm_b32 v182, v49, v48, s64\n"
    "v_perm_b32 v183, v49, v48, s69\n"
    "s_add_u32 m0, s66, 0x6000\n"
    "s_add_u32 s61, s59, 0x220000\n"
    "buffer_load_dwordx4 v67, s[80:83], s61 offen lds\n"
    "ds_read2st64_b32 v[18:19], v73 offset0:50 offset1:54\n"
    "ds_read2st64_b32 v[20:21], v73 offset0:58 offset1:62\n"
    "v_perm_b32 v208, v51, v50, s64\n"
    "v_perm_b32 v209, v51, v50, s69\n"
    "v_perm_b32 v210, v53, v52, s64\n"
    "v_perm_b32 v211, v53, v52, s69\n"
    "s_add_u32 m0, s66, 0x6400\n"
    "s_add_u32 s61, s59, 0x228000\n"
    "buffer_load_dwordx4 v67, s[80:83], s61 offen lds\n"
    "ds_read2st64_b32 v[22:23], v74 offset0:50 offset1:54\n"
    "ds_read2st64_b32 v[24:25], v74 offset0:58 offset1:62\n"
    "v_perm_b32 v212, v55, v54, s64\n"
    "v_perm_b32 v213, v55, v54, s69\n"
    "v_perm_b32 v214, v57, v56, s64\n"
    "v_perm_b32 v215, v57, v56, s69\n"
    "s_add_u32 m0, s66, 0x6800\n"
    "s_add_u32 s61, s59, 0x230000\n"
    "buffer_load_dwordx4 v68, s[80:83], s61 offen lds\n"
    "ds_read2st64_b32 v[26:27], v73 offset0:51 offset1:55\n"
    "ds_read2st64_b32 v[28:29], v73 offset0:59 offset1:63\n"
    "v_perm_b32 v240, v59, v58, s64\n"
    "v_perm_b32 v241, v59, v58, s69\n"
    "v_perm_b32 v242, v61, v60, s64\n"
    "v_perm_b32 v243, v61, v60, s69\n"
    "s_add_u32 m0, s66, 0x6c00\n"
    "s_add_u32 s61, s59, 0x238000\n"
    "buffer_load_dwordx4 v68, s[80:83], s61 offen lds\n"
    "ds_read2st64_b32 v[30:31], v74 offset0:51 offset1:55\n"
    "ds_read2st64_b32 v[32:33], v74 offset0:59 offset1:63\n"
    "v_perm_b32 v244, v63, v62, s64\n"
    "v_perm_b32 v245, v63, v62, s69\n"
    "v_perm_b32 v246, v65, v64, s64\n"
    "v_perm_b32 v247, v65, v64, s69\n"
    "s_waitcnt vmcnt(16)\n"
    "s_waitcnt lgkmcnt(0)\n"
    "s_barrier\n"
    "v_lshlrev_b32 v34, 5, v0\n"
    "s_add_u32 s62, s57, s3\n"
    "v_lshrrev_b32 v34, s99, v34\n"
    "buffer_load_dwordx2 v[108:109], v34, s[12:15], s62 offen sc1\n"
    "s_add_u32 s62, s62, s98\n"
    "buffer_load_dwordx2 v[110:111], v34, s[12:15], s62 offen offset:256 sc1\n"
    "ds_read_b128 v[100:103], v0 offset:29760\n"
    "s_add_u32 m0, s66, 0x3000\n"
    "s_add_u32 s61, s59, 0x300000\n"
    "buffer_load_dwordx4 v67, s[80:83], s61 offen lds\n"
    "ds_read_b64_tr_b16 a[0:1], v69 offset:0\n"
    "ds_read_b64_tr_b16 a[2:3], v69 offset:1024\n"
    "v_perm_b32 v152, v3, v2, s64\n"
    "v_perm_b32 v153, v3, v2, s69\n"
    "v_perm_b32 v154, v5, v4, s64\n"
    "v_perm_b32 v155, v5, v4, s69\n"
    "s_add_u32 m0, s66, 0x3400\n"
    "s_add_u32 s61, s59, 0x308000\n"
    "buffer_load_dwordx4 v67, s[80:83], s61 offen lds\n"
    "ds_read_b64_tr_b16 a[4:5], v69 offset:32768\n"
    "ds_read_b64_tr_b16 a[6:7], v69 offset:33792\n"
    "v_perm_b32 v156, v7, v6, s64\n"
    "v_perm_b32 v157, v7, v6, s69\n"
    "v_perm_b32 v158, v9, v8, s64\n"
    "v_perm_b32 v159, v9, v8, s69\n"
    "s_add_u32 m0, s66, 0x3800\n"
    "s_add_u32 s61, s59, 0x310000\n"
    "buffer_load_dwordx4 v68, s[80:83], s61 offen lds\n"
    "ds_read_b64_tr_b16 a[8:9], v71 offset:0\n"
    "ds_read_b64_tr_b16 a[10:11], v71 offset:1024\n"
    "v_perm_b32 v184, v11, v10, s64\n"
    "v_perm_b32 v185, v11, v10, s69\n"
    "v_perm_b32 v186, v13, v12, s64\n"
    "v_perm_b32 v187, v13, v12, s69\n"
    "s_add_u32 m0, s66, 0x3c00\n"
    "s_add_u32 s61, s59, 0x318000\n"
    "buffer_load_dwordx4 v68, s[80:83], s61 offen lds\n"
    "ds_read_b64_tr_b16 a[12:13], v71 offset:32768\n"
    "ds_read_b64_tr_b16 a[14:15], v71 offset:33792\n"
    "v_perm_b32 v188, v15, v14, s64\n"
    "v_perm_b32 v189, v15, v14, s69\n"
    "v_perm_b32 v190, v17, v16, s64\n"
    "v_perm_b32 v191, v17, v16, s69\n"
    "s_add_u32 m0, s66, 0x7000\n"
    "s_add_u32 s61, s59, 0x320000\n"
    "buffer_load_dwordx4 v67, s[80:83], s61 offen lds\n"
    "ds_read_b64_tr_b16 a[64:65], v70 offset:0\n"
    "ds_read_b64_tr_b16 a[66:67], v70 offset:1024\n"
    "v_perm_b32 v216, v19, v18, s64\n"
    "v_perm_b32 v217, v19, v18, s69\n"
    "v_perm_b32 v218, v21, v20, s64\n"
    "v_perm_b32 v219, v21, v20, s69\n"
    "s_add_u32 m0, s66, 0x7400\n"
    "s_add_u32 s61, s59, 0x328000\n"
    "buffer_load_dwordx4 v67, s[80:83], s61 offen lds\n"
    "ds_read_b64_tr_b16 a[68:69], v70 offset:32768\n"
    "ds_read_b64_tr_b16 a[70:71], v70 offset:33792\n"
    "v_perm_b32 v220, v23, v22, s64\n"
    "v_perm_b32 v221, v23, v22, s69\n"
    "v_perm_b32 v222, v25, v24, s64\n"
    "v_perm_b32 v223, v25, v24, s69\n"
    "s_add_u32 m0, s66, 0x7800\n"
    "s_add_u32 s61, s59, 0x330000\n"
    "buffer_load_dwordx4 v68, s[80:83], s61 offen lds\n"
    "ds_read_b64_tr_b16 a[72:73], v72 offset:0\n"
    "ds_read_b64_tr_b16 a[74:75], v72 offset:1024\n"
    "v_perm_b32 v248, v27, v26, s64\n"
    "v_perm_b32 v249, v27, v26, s69\n"
    "v_perm_b32 v250, v29, v28, s64\n"
    "v_perm_b32 v251, v29, v28, s69\n"
    "s_add_u32 m0, s66, 0x7c00\n"
    "s_add_u32 s61, s59, 0x338000\n"
    "buffer_load_dwordx4 v68, s[80:83], s61 offen lds\n"
    "ds_read_b64_tr_b16 a[76:77], v72 offset:32768\n"
    "ds_read_b64_tr_b16 a[78:79], v72 offset:33792\n"
    "v_perm_b32 v252, v31, v30, s64\n"
    "v_perm_b32 v253, v31, v30, s69\n"
    "v_perm_b32 v254, v33, v32, s64\n"
    "v_perm_b32 v255, v33, v32, s69\n"
    "s_waitcnt vmcnt(18)\n"
    "s_waitcnt lgkmcnt(0)\n"
    "s_barrier\n"
    "s_mov_b32 s80, s74\n"
    "s_and_b32 s81, s75, 0xffff\n"
    "s_add_u32 m0, s66, 0\n"
    "s_add_u32 s61, s59, 0\n"
    "buffer_load_dwordx4 v67, s[80:83], s61 offen lds\n"
    "ds_read_b64_tr_b16 a[16:17], v69 offset:4096\n"
    "ds_read_b64_tr_b16 a[18:19], v69 offset:5120\n"
    "s_add_u32 m0, s66, 0x400\n"
    "s_add_u32 s61, s59, 0x8000\n"
    "buffer_load_dwordx4 v67, s[80:83], s61 offen lds\n"
    "ds_read_b64_tr_b16 a[20:21], v69 offset:36864\n"
    "ds_read_b64_tr_b16 a[22:23], v69 offset:37888\n"
    "s_add_u32 m0, s66, 0x800\n"
    "s_add_u32 s61, s59, 0x10000\n"
    "buffer_load_dwordx4 v68, s[80:83], s61 offen lds\n"
    "ds_read_b64_tr_b16 a[24:25], v71 offset:4096\n"
    "ds_read_b64_tr_b16 a[26:27], v71 offset:5120\n"
    "s_add_u32 m0, s66, 0xc00\n"
    "s_add_u32 s61, s59, 0x18000\n"
    "buffer_load_dwordx4 v68, s[80:83], s61 offen lds\n"
    "ds_read_b64_tr_b16 a[28:29], v71 offset:36864\n"
    "ds_read_b64_tr_b16 a[30:31], v71 offset:37888\n"
    "s_add_u32 m0, s66, 0x4000\n"
    "s_add_u32 s61, s59, 0x20000\n"
    "buffer_load_dwordx4 v67, s[80:83], s61 offen lds\n"
    "ds_read_b64_tr_b16 a[80:81], v70 offset:4096\n"
    "ds_read_b64_tr_b16 a[82:83], v70 offset:5120\n"
    "s_add_u32 m0, s66, 0x4400\n"
    "s_add_u32 s61, s59, 0x28000\n"
    "buffer_load_dwordx4 v67, s[80:83], s61 offen lds\n"
    "ds_read_b64_tr_b16 a[84:85], v70 offset:36864\n"
    "ds_read_b64_tr_b16 a[86:87], v70 offset:37888\n"
    "s_add_u32 m0, s66, 0x4800\n"
    "s_add_u32 s61, s59, 0x30000\n"
    "buffer_load_dwordx4 v68, s[80:83], s61 offen lds\n"
    "ds_read_b64_tr_b16 a[88:89], v72 offset:4096\n"
    "ds_read_b64_tr_b16 a[90:91], v72 offset:5120\n"
    "s_add_u32 m0, s66, 0x4c00\n"
    "s_add_u32 s61, s59, 0x38000\n"
    "buffer_load_dwordx4 v68, s[80:83], s61 offen lds\n"
    "ds_read_b64_tr_b16 a[92:93], v72 offset:36864\n"
    "ds_read_b64_tr_b16 a[94:95], v72 offset:37888\n"
    "s_waitcnt vmcnt(18)\n"
    "s_waitcnt lgkmcnt(0)\n"
    "s_barrier\n"
    "s_add_u32 m0, s66, 0x1000\n"
    "s_add_u32 s61, s59, 0x100000\n"
    "buffer_load_dwordx4 v67, s[80:83], s61 offen lds\n"
    "ds_read_b64_tr_b16 a[32:33], v69 offset:8192\n"
    "ds_read_b64_tr_b16 a[34:35], v69 offset:9216\n"
    "s_add_u32 m0, s66, 0x1400\n"
    "s_add_u32 s61, s59, 0x108000\n"
    "buffer_load_dwordx4 v67, s[80:83], s61 offen lds\n"
    "ds_read_b64_tr_b16 a[36:37], v69 offset:40960\n"
    "ds_read_b64_tr_b16 a[38:39], v69 offset:41984\n"
    "s_add_u32 m0, s66, 0x1800\n"
    "s_add_u32 s61, s59, 0x110000\n"
    "buffer_load_dwordx4 v68, s[80:83], s61 offen lds\n"
    "ds_read_b64_tr_b16 a[40:41], v71 offset:8192\n"
    "ds_read_b64_tr_b16 a[42:43], v71 offset:9216\n"
    "s_add_u32 m0, s66, 0x1c00\n"
    "s_add_u32 s61, s59, 0x118000\n"
    "buffer_load_dwordx4 v68, s[80:83], s61 offen lds\n"
    "ds_read_b64_tr_b16 a[44:45], v71 offset:40960\n"
    "ds_read_b64_tr_b16 a[46:47], v71 offset:41984\n"
    "s_add_u32 m0, s66, 0x5000\n"
    "s_add_u32 s61, s59, 0x120000\n"
    "buffer_load_dwordx4 v67, s[80:83], s61 offen lds\n"
    "ds_read_b64_tr_b16 a[96:97], v70 offset:8192\n"
    "ds_read_b64_tr_b16 a[98:99], v70 offset:9216\n"
    "s_add_u32 m0, s66, 0x5400\n"
    "s_add_u32 s61, s59, 0x128000\n"
    "buffer_load_dwordx4 v67, s[80:83], s61 offen lds\n"
    "ds_read_b64_tr_b16 a[100:101], v70 offset:40960\n"
    "ds_read_b64_tr_b16 a[102:103], v70 offset:41984\n"
    "s_add_u32 m0, s66, 0x5800\n"
    "s_add_u32 s61, s59, 0x130000\n"
    "buffer_load_dwordx4 v68, s[80:83], s61 offen lds\n"
    "ds_read_b64_tr_b16 a[104:105], v72 offset:8192\n"
    "ds_read_b64_tr_b16 a[106:107], v72 offset:9216\n"
    "s_add_u32 m0, s66, 0x5c00\n"
    "s_add_u32 s61, s59, 0x138000\n"
    "buffer_load_dwordx4 v68, s[80:83], s61 offen lds\n"
    "ds_read_b64_tr_b16 a[108:109], v72 offset:40960\n"
    "ds_read_b64_tr_b16 a[110:111], v72 offset:41984\n"
    "s_waitcnt vmcnt(16)\n"
    "s_waitcnt lgkmcnt(0)\n"
    "s_barrier\n"
    "s_add_u32 m0, s66, 0x2000\n"
    "s_add_u32 s61, s59, 0x200000\n"
    "buffer_load_dwordx4 v67, s[80:83], s61 offen lds\n"
    "ds_read_b64_tr_b16 a[48:49], v69 offset:12288\n"
    "ds_read_b64_tr_b16 a[50:51], v69 offset:13312\n"
    "v_lshlrev_b32 v120, 16, v116\n"
    "v_and_b32 v121, 0xffff0000, v116\n"
    "v_lshlrev_b32 v122, 16, v117\n"
    "v_and_b32 v123, 0xffff0000, v117\n"
    "v_lshlrev_b32 v124, 16, v118\n"
    "s_add_u32 m0, s66, 0x2400\n"
    "s_add_u32 s61, s59, 0x208000\n"
    "buffer_load_dwordx4 v67, s[80:83], s61 offen lds\n"
    "ds_read_b64_tr_b16 a[52:53], v69 offset:45056\n"
    "ds_read_b64_tr_b16 a[54:55], v69 offset:46080\n"
    "v_and_b32 v125, 0xffff0000, v118\n"
    "v_lshlrev_b32 v126, 16, v119\n"
    "v_and_b32 v127, 0xffff0000, v119\n"
    "v_max3_u32 v6, v108, v109, v110\n"
    "v_max_u32 v6, v6, v111\n"
    "s_add_u32 m0, s66, 0x2800\n"
    "s_add_u32 s61, s59, 0x210000\n"
    "buffer_load_dwordx4 v68, s[80:83], s61 offen lds\n"
    "ds_read_b64_tr_b16 a[56:57], v71 offset:12288\n"
    "ds_read_b64_tr_b16 a[58:59], v71 offset:13312\n"
    "v_cmp_eq_u32 vcc, -1, v6\n"
    "s_cbranch_vccnz r0chk_%=\n"
    "r0ok_%=:\n"
    "v_pk_add_f32 v[4:5], v[120:121], v[122:123]\n"
    "v_pk_add_f32 v[2:3], v[124:125], v[126:127]\n"
    "s_add_u32 m0, s66, 0x2c00\n"
    "s_add_u32 s61, s59, 0x218000\n"
    "buffer_load_dwordx4 v68, s[80:83], s61 offen lds\n"
    "ds_read_b64_tr_b16 a[60:61], v71 offset:45056\n"
    "ds_read_b64_tr_b16 a[62:63], v71 offset:46080\n"
    "v_pk_add_f32 v[4:5], v[4:5], v[2:3]\n"
    "v_add_f32 v4, v4, v5\n"
    "v_lshlrev_b32 v112, 16, v108\n"
    "v_and_b32 v113, 0xffff0000, v108\n"
    "v_add_f32_dpp v4, v4, v4 row_ror:8 row_mask:0xf bank_mask:0xf\n"
    "s_add_u32 m0, s66, 0x6000\n"
    "s_add_u32 s61, s59, 0x220000\n"
    "buffer_load_dwordx4 v67, s[80:83], s61 offen lds\n"
    "ds_read_b64_tr_b16 a[112:113], v70 offset:12288\n"
    "ds_read_b64_tr_b16 a[114:115], v70 offset:13312\n"
    "v_lshlrev_b32 v114, 16, v109\n"
    "v_and_b32 v115, 0xffff0000, v109\n"
    "v_add_f32_dpp v4, v4, v4 row_ror:4 row_mask:0xf bank_mask:0xf\n"
    "v_lshlrev_b32 v116, 16, v110\n"
    "v_and_b32 v117, 0xffff0000, v110\n"
    "s_add_u32 m0, s66, 0x6400\n"
    "s_add_u32 s61, s59, 0x228000\n"
    "buffer_load_dwordx4 v67, s[80:83], s61 offen lds\n"
    "ds_read_b64_tr_b16 a[116:117], v70 offset:45056\n"
    "ds_read_b64_tr_b16 a[118:119], v70 offset:46080\n"
    "v_add_f32_dpp v4, v4, v4 row_ror:2 row_mask:0xf bank_mask:0xf\n"
    "v_lshlrev_b32 v118, 16, v111\n"
    "v_and_b32 v119, 0xffff0000, v111\n"
    "v_add_f32_dpp v4, v4, v4 row_ror:1 row_mask:0xf bank_mask:0xf\n"
    "v_mov_b32 v5, v4\n"
    "s_add_u32 m0, s66, 0x6800\n"
    "s_add_u32 s61, s59, 0x230000\n"
    "buffer_load_dwordx4 v68, s[80:83], s61 offen lds\n"
    "ds_read_b64_tr_b16 a[120:121], v72 offset:12288\n"
    "ds_read_b64_tr_b16 a[122:123], v72 offset:13312\n"
    "v_permlane32_swap_b32 v4, v5\n"
    "v_add_f32 v4, v4, v5\n"
    "v_mov_b32 v5, v4\n"
    "s_nop 1\n"
    "v_permlane16_swap_b32 v4, v5\n"
    "v_add_f32 v4, v4, v5\n"
    "s_add_u32 m0, s66, 0x6c00\n"
    "s_add_u32 s61, s59, 0x238000\n"
    "buffer_load_dwordx4 v68, s[80:83], s61 offen lds\n"
    "ds_read_b64_tr_b16 a[124:125], v72 offset:45056\n"
    "ds_read_b64_tr_b16 a[126:127], v72 offset:46080\n"
    "v_mul_f32 v4, 0x3b000000, v4\n"
    "v_pk_add_f32 v[104:105], v[120:121], v[4:5] op_sel_hi:[1,0] neg_lo:[0,1] neg_hi:[0,1]\n"
    "v_pk_add_f32 v[106:107], v[122:123], v[4:5] op_sel_hi:[1,0] neg_lo:[0,1] neg_hi:[0,1]\n"
    "v_pk_add_f32 v[108:109], v[124:125], v[4:5] op_sel_hi:[1,0] neg_lo:[0,1] neg_hi:[0,1]\n"
    "v_pk_add_f32 v[110:111], v[126:127], v[4:5] op_sel_hi:[1,0] neg_lo:[0,1] neg_hi:[0,1]\n"
    "s_waitcnt vmcnt(16)\n"
    "s_waitcnt lgkmcnt(0)\n"
    "s_barrier\n"
    "s_add_u32 m0, s66, 0x3000\n"
    "s_add_u32 s61, s59, 0x300000\n"
    "buffer_load_dwordx4 v67, s[80:83], s61 offen lds\n"
    "ds_read_b64_tr_b16 a[128:129], v69 offset:0\n"
    "ds_read_b64_tr_b16 a[130:131], v69 offset:1024\n"
    "v_pk_mul_f32 v[2:3], v[104:105], v[104:105]\n"
    "v_pk_fma_f32 v[2:3], v[106:107], v[106:107], v[2:3]\n"
    "v_pk_fma_f32 v[2:3], v[108:109], v[108:109], v[2:3]\n"
    "v_pk_fma_f32 v[2:3], v[110:111], v[110:111], v[2:3]\n"
    "v_add_f32 v2, v2, v3\n"
    "s_add_u32 m0, s66, 0x3400\n"
    "s_add_u32 s61, s59, 0x308000\n"
    "buffer_load_dwordx4 v67, s[80:83], s61 offen lds\n"
    "ds_read_b64_tr_b16 a[132:133], v69 offset:32768\n"
    "ds_read_b64_tr_b16 a[134:135], v69 offset:33792\n"
    "v_cvt_pk_bf16_f32 v4, v4, v4\n"
    "v_and_b32 v4, 0xffff0000, v4\n"
    "v_add_f32_dpp v2, v2, v2 row_ror:8 row_mask:0xf bank_mask:0xf\n"
    "v_pk_add_f32 v[104:105], v[120:121], v[4:5] op_sel_hi:[1,0] neg_lo:[0,1] neg_hi:[0,1]\n"
    "v_pk_add_f32 v[106:107], v[122:123], v[4:5] op_sel_hi:[1,0] neg_lo:[0,1] neg_hi:[0,1]\n"
    "s_add_u32 m0, s66, 0x3800\n"
    "s_add_u32 s61, s59, 0x310000\n"
    "buffer_load_dwordx4 v68, s[80:83], s61 offen lds\n"
    "ds_read_b64_tr_b16 a[136:137], v71 offset:0\n"
    "ds_read_b64_tr_b16 a[138:139], v71 offset:1024\n"
    "v_add_f32_dpp v2, v2, v2 row_ror:4 row_mask:0xf bank_mask:0xf\n"
    "v_pk_add_f32 v[108:109], v[124:125], v[4:5] op_sel_hi:[1,0] neg_lo:[0,1] neg_hi:[0,1]\n"
    "v_pk_add_f32 v[110:111], v[126:127], v[4:5] op_sel_hi:[1,0] neg_lo:[0,1] neg_hi:[0,1]\n"
    "v_add_f32_dpp v2, v2, v2 row_ror:2 row_mask:0xf bank_mask:0xf\n"
    "v_cvt_pk_bf16_f32 v96, v104, v105\n"
    "s_add_u32 m0, s66, 0x3c00\n"
    "s_add_u32 s61, s59, 0x318000\n"
    "buffer_load_dwordx4 v68, s[80:83], s61 offen lds\n"
    "ds_read_b64_tr_b16 a[140:141], v71 offset:32768\n"
    "ds_read_b64_tr_b16 a[142:143], v71 offset:33792\n"
    "v_cvt_pk_bf16_f32 v97, v106, v107\n"
    "v_add_f32_dpp v2, v2, v2 row_ror:1 row_mask:0xf bank_mask:0xf\n"
    "v_mov_b32 v3, v2\n"
    "v_cvt_pk_bf16_f32 v98, v108, v109\n"
    "v_cvt_pk_bf16_f32 v99, v110, v111\n"
    "s_add_u32 m0, s66, 0x7000\n"
    "s_add_u32 s61, s59, 0x320000\n"
    "buffer_load_dwordx4 v67, s[80:83], s61 offen lds\n"
    "ds_read_b64_tr_b16 a[192:193], v70 offset:0\n"
    "ds_read_b64_tr_b16 a[194:195], v70 offset:1024\n"
    "v_permlane32_swap_b32 v2, v3\n"
    "v_add_f32 v2, v2, v3\n"
    "v_mov_b32 v3, v2\n"
    "v_lshlrev_b32 v104, 16, v96\n"
    "v_and_b32 v105, 0xffff0000, v96\n"
    "s_add_u32 m0, s66, 0x7400\n"
    "s_add_u32 s61, s59, 0x328000\n"
    "buffer_load_dwordx4 v67, s[80:83], s61 offen lds\n"
    "ds_read_b64_tr_b16 a[196:197], v70 offset:32768\n"
    "ds_read_b64_tr_b16 a[198:199], v70 offset:33792\n"
    "v_permlane16_swap_b32 v2, v3\n"
    "v_add_f32 v2, v2, v3\n"
    "v_mul_f32 v2, 0x3b000000, v2\n"
    "v_cvt_pk_bf16_f32 v2, v2, v2\n"
    "v_and_b32 v2, 0xffff0000, v2\n"
    "s_add_u32 m0, s66, 0x7800\n"
    "s_add_u32 s61, s59, 0x330000\n"
    "buffer_load_dwordx4 v68, s[80:83], s61 offen lds\n"
    "ds_read_b64_tr_b16 a[200:201], v72 offset:0\n"
    "ds_read_b64_tr_b16 a[202:203], v72 offset:1024\n"
    "v_add_f32 v2, 0x3727c5ac, v2\n"
    "v_cvt_pk_bf16_f32 v2, v2, v2\n"
    "v_and_b32 v2, 0xffff0000, v2\n"
    "v_sqrt_f32 v2, v2\n"
    "v_lshlrev_b32 v106, 16, v97\n"
    "s_add_u32 m0, s66, 0x7c00\n"
    "s_add_u32 s61, s59, 0x338000\n"
    "buffer_load_dwordx4 v68, s[80:83], s61 offen lds\n"
    "ds_read_b64_tr_b16 a[204:205], v72 offset:32768\n"
    "ds_read_b64_tr_b16 a[206:207], v72 offset:33792\n"
    "v_and_b32 v107, 0xffff0000, v97\n"
    "v_cvt_pk_bf16_f32 v2, v2, v2\n"
    "v_and_b32 v2, 0xffff0000, v2\n"
    "v_rcp_f32 v2, v2\n"
    "v_lshlrev_b32 v108, 16, v98\n"
    "s_waitcnt vmcnt(16)\n"
    "s_waitcnt lgkmcnt(0)\n"
    "s_barrier\n"
    "s_mov_b32 s80, s76\n"
    "s_and_b32 s81, s77, 0xffff\n"
    "s_add_u32 m0, s66, 0\n"
    "s_add_u32 s61, s59, 0\n"
    "buffer_load_dwordx4 v67, s[80:83], s61 offen lds\n"
    "ds_read_b64_tr_b16 a[144:145], v69 offset:4096\n"
    "ds_read_b64_tr_b16 a[146:147], v69 offset:5120\n"
    "v_and_b32 v109, 0xffff0000, v98\n"
    "v_lshlrev_b32 v110, 16, v99\n"
    "v_and_b32 v111, 0xffff0000, v99\n"
    "v_pk_mul_f32 v[104:105], v[104:105], v[2:3] op_sel_hi:[1,0]\n"
    "v_pk_mul_f32 v[106:107], v[106:107], v[2:3] op_sel_hi:[1,0]\n"
    "s_add_u32 m0, s66, 0x400\n"
    "s_add_u32 s61, s59, 0x8000\n"
    "buffer_load_dwordx4 v67, s[80:83], s61 offen lds\n"
    "ds_read_b64_tr_b16 a[148:149], v69 offset:36864\n"
    "ds_read_b64_tr_b16 a[150:151], v69 offset:37888\n"
    "v_pk_mul_f32 v[108:109], v[108:109], v[2:3] op_sel_hi:[1,0]\n"
    "v_pk_mul_f32 v[110:111], v[110:111], v[2:3] op_sel_hi:[1,0]\n"
    "v_cvt_pk_bf16_f32 v96, v104, v105\n"
    "v_cvt_pk_bf16_f32 v97, v106, v107\n"
    "v_cvt_pk_bf16_f32 v98, v108, v109\n"
    "s_add_u32 m0, s66, 0x800\n"
    "s_add_u32 s61, s59, 0x10000\n"
    "buffer_load_dwordx4 v68, s[80:83], s61 offen lds\n"
    "ds_read_b64_tr_b16 a[152:153], v71 offset:4096\n"
    "ds_read_b64_tr_b16 a[154:155], v71 offset:5120\n"
    "v_cvt_pk_bf16_f32 v99, v110, v111\n"
    "v_lshlrev_b32 v104, 16, v96\n"
    "v_and_b32 v105, 0xffff0000, v96\n"
    "v_lshlrev_b32 v106, 16, v97\n"
    "v_and_b32 v107, 0xffff0000, v97\n"
    "s_add_u32 m0, s66, 0xc00\n"
    "s_add_u32 s61, s59, 0x18000\n"
    "buffer_load_dwordx4 v68, s[80:83], s61 offen lds\n"
    "ds_read_b64_tr_b16 a[156:157], v71 offset:36864\n"
    "ds_read_b64_tr_b16 a[158:159], v71 offset:37888\n"
    "v_lshlrev_b32 v108, 16, v98\n"
    "v_and_b32 v109, 0xffff0000, v98\n"
    "v_lshlrev_b32 v110, 16, v99\n"
    "v_and_b32 v111, 0xffff0000, v99\n"
    "v_pk_mul_f32 v[104:105], v[104:105], v[112:113]\n"
    "s_add_u32 m0, s66, 0x4000\n"
    "s_add_u32 s61, s59, 0x20000\n"
    "buffer_load_dwordx4 v67, s[80:83], s61 offen lds\n"
    "ds_read_b64_tr_b16 a[208:209], v70 offset:4096\n"
    "ds_read_b64_tr_b16 a[210:211], v70 offset:5120\n"
    "v_pk_mul_f32 v[106:107], v[106:107], v[114:115]\n"
    "v_pk_mul_f32 v[108:109], v[108:109], v[116:117]\n"
    "v_pk_mul_f32 v[110:111], v[110:111], v[118:119]\n"
    "v_cvt_pk_bf16_f32 v96, v104, v105\n"
    "v_cvt_pk_bf16_f32 v97, v106, v107\n"
    "s_add_u32 m0, s66, 0x4400\n"
    "s_add_u32 s61, s59, 0x28000\n"
    "buffer_load_dwordx4 v67, s[80:83], s61 offen lds\n"
    "ds_read_b64_tr_b16 a[212:213], v70 offset:36864\n"
    "ds_read_b64_tr_b16 a[214:215], v70 offset:37888\n"
    "v_cvt_pk_bf16_f32 v98, v108, v109\n"
    "v_cvt_pk_bf16_f32 v99, v110, v111\n"
    "v_lshlrev_b32 v104, 16, v96\n"
    "v_and_b32 v105, 0xffff0000, v96\n"
    "v_lshlrev_b32 v106, 16, v97\n"
    "s_add_u32 m0, s66, 0x4800\n"
    "s_add_u32 s61, s59, 0x30000\n"
    "buffer_load_dwordx4 v68, s[80:83], s61 offen lds\n"
    "ds_read_b64_tr_b16 a[216:217], v72 offset:4096\n"
    "ds_read_b64_tr_b16 a[218:219], v72 offset:5120\n"
    "v_and_b32 v107, 0xffff0000, v97\n"
    "v_lshlrev_b32 v108, 16, v98\n"
    "v_and_b32 v109, 0xffff0000, v98\n"
    "v_lshlrev_b32 v110, 16, v99\n"
    "v_and_b32 v111, 0xffff0000, v99\n"
    "s_add_u32 m0, s66, 0x4c00\n"
    "s_add_u32 s61, s59, 0x38000\n"
    "buffer_load_dwordx4 v68, s[80:83], s61 offen lds\n"
    "ds_read_b64_tr_b16 a[220:221], v72 offset:36864\n"
    "ds_read_b64_tr_b16 a[222:223], v72 offset:37888\n"
    "v_pk_add_f32 v[120:121], v[120:121], v[104:105]\n"
    "v_pk_add_f32 v[122:123], v[122:123], v[106:107]\n"
    "v_pk_add_f32 v[124:125], v[124:125], v[108:109]\n"
    "v_pk_add_f32 v[126:127], v[126:127], v[110:111]\n"
    "v_cvt_pk_bf16_f32 v116, v120, v121\n"
    "s_waitcnt vmcnt(16)\n"
    "s_waitcnt lgkmcnt(0)\n"
    "s_barrier\n"
    "s_add_u32 m0, s66, 0x1000\n"
    "s_add_u32 s61, s59, 0x100000\n"
    "buffer_load_dwordx4 v67, s[80:83], s61 offen lds\n"
    "ds_read_b64_tr_b16 a[160:161], v69 offset:8192\n"
    "ds_read_b64_tr_b16 a[162:163], v69 offset:9216\n"
    "v_cvt_pk_bf16_f32 v117, v122, v123\n"
    "v_cvt_pk_bf16_f32 v118, v124, v125\n"
    "v_cvt_pk_bf16_f32 v119, v126, v127\n"
    "s_add_u32 s62, s57, s36\n"
    "buffer_store_dwordx4 v[116:119], v0, s[12:15], s62 offen sc1\n"
    "s_add_u32 m0, s66, 0x1400\n"
    "s_add_u32 s61, s59, 0x108000\n"
    "buffer_load_dwordx4 v67, s[80:83], s61 offen lds\n"
    "ds_read_b64_tr_b16 a[164:165], v69 offset:40960\n"
    "ds_read_b64_tr_b16 a[166:167], v69 offset:41984\n"
    "v_lshlrev_b32 v120, 16, v116\n"
    "v_and_b32 v121, 0xffff0000, v116\n"
    "v_lshlrev_b32 v122, 16, v117\n"
    "v_and_b32 v123, 0xffff0000, v117\n"
    "v_lshlrev_b32 v124, 16, v118\n"
    "s_add_u32 m0, s66, 0x1800\n"
    "s_add_u32 s61, s59, 0x110000\n"
    "buffer_load_dwordx4 v68, s[80:83], s61 offen lds\n"
    "ds_read_b64_tr_b16 a[168:169], v71 offset:8192\n"
    "ds_read_b64_tr_b16 a[170:171], v71 offset:9216\n"
    "v_and_b32 v125, 0xffff0000, v118\n"
    "v_lshlrev_b32 v126, 16, v119\n"
    "v_and_b32 v127, 0xffff0000, v119\n"
    "v_pk_add_f32 v[2:3], v[120:121], v[122:123]\n"
    "v_pk_add_f32 v[4:5], v[124:125], v[126:127]\n"
    "s_add_u32 m0, s66, 0x1c00\n"
    "s_add_u32 s61, s59, 0x118000\n"
    "buffer_load_dwordx4 v68, s[80:83], s61 offen lds\n"
    "ds_read_b64_tr_b16 a[172:173], v71 offset:40960\n"
    "ds_read_b64_tr_b16 a[174:175], v71 offset:41984\n"
    "v_pk_add_f32 v[2:3], v[2:3], v[4:5]\n"
    "v_add_f32 v2, v2, v3\n"
    "s_nop 1\n"
    "v_add_f32_dpp v2, v2, v2 row_ror:8 row_mask:0xf bank_mask:0xf\n"
    "s_nop 1\n"
    "v_add_f32_dpp v2, v2, v2 row_ror:4 row_mask:0xf bank_mask:0xf\n"
    "s_nop 1\n"
    "v_add_f32_dpp v2, v2, v2 row_ror:2 row_mask:0xf bank_mask:0xf\n"
    "s_add_u32 m0, s66, 0x5000\n"
    "s_add_u32 s61, s59, 0x120000\n"
    "buffer_load_dwordx4 v67, s[80:83], s61 offen lds\n"
    "ds_read_b64_tr_b16 a[224:225], v70 offset:8192\n"
    "ds_read_b64_tr_b16 a[226:227], v70 offset:9216\n"
    "v_add_f32_dpp v2, v2, v2 row_ror:1 row_mask:0xf bank_mask:0xf\n"
    "v_mov_b32 v3, v2\n"
    "s_nop 1\n"
    "v_permlane32_swap_b32 v2, v3\n"
    "v_add_f32 v2, v2, v3\n"
    "v_mov_b32 v3, v2\n"
    "s_add_u32 m0, s66, 0x5400\n"
    "s_add_u32 s61, s59, 0x128000\n"
    "buffer_load_dwordx4 v67, s[80:83], s61 offen lds\n"
    "ds_read_b64_tr_b16 a[228:229], v70 offset:40960\n"
    "ds_read_b64_tr_b16 a[230:231], v70 offset:41984\n"
    "v_permlane16_swap_b32 v2, v3\n"
    "v_add_f32 v2, v2, v3\n"
    "v_mul_f32 v2, 0x3b000000, v2\n"
    "v_pk_add_f32 v[104:105], v[120:121], v[2:3] op_sel_hi:[1,0] neg_lo:[0,1] neg_hi:[0,1]\n"
    "v_pk_add_f32 v[106:107], v[122:123], v[2:3] op_sel_hi:[1,0] neg_lo:[0,1] neg_hi:[0,1]\n"
    "s_add_u32 m0, s66, 0x5800\n"
    "s_add_u32 s61, s59, 0x130000\n"
    "buffer_load_dwordx4 v68, s[80:83], s61 offen lds\n"
    "ds_read_b64_tr_b16 a[232:233], v72 offset:8192\n"
    "ds_read_b64_tr_b16 a[234:235], v72 offset:9216\n"
    "v_pk_add_f32 v[108:109], v[124:125], v[2:3] op_sel_hi:[1,0] neg_lo:[0,1] neg_hi:[0,1]\n"
    "v_pk_add_f32 v[110:111], v[126:127], v[2:3] op_sel_hi:[1,0] neg_lo:[0,1] neg_hi:[0,1]\n"
    "v_pk_mul_f32 v[4:5], v[104:105], v[104:105]\n"
    "v_pk_fma_f32 v[4:5], v[106:107], v[106:107], v[4:5]\n"
    "v_pk_fma_f32 v[4:5], v[108:109], v[108:109], v[4:5]\n"
    "s_add_u32 m0, s66, 0x5c00\n"
    "s_add_u32 s61, s59, 0x138000\n"
    "buffer_load_dwordx4 v68, s[80:83], s61 offen lds\n"
    "ds_read_b64_tr_b16 a[236:237], v72 offset:40960\n"
    "ds_read_b64_tr_b16 a[238:239], v72 offset:41984\n"
    "v_pk_fma_f32 v[4:5], v[110:111], v[110:111], v[4:5]\n"
    "v_add_f32 v4, v4, v5\n"
    "v_cvt_pk_bf16_f32 v2, v2, v2\n"
    "v_and_b32 v2, 0xffff0000, v2\n"
    "v_add_f32_dpp v4, v4, v4 row_ror:8 row_mask:0xf bank_mask:0xf\n"
    "s_waitcnt vmcnt(17)\n"
    "s_waitcnt lgkmcnt(0)\n"
    "s_barrier\n"
    "s_add_u32 m0, s66, 0x2000\n"
    "s_add_u32 s61, s59, 0x200000\n"
    "buffer_load_dwordx4 v67, s[80:83], s61 offen lds\n"
    "ds_read_b64_tr_b16 a[176:177], v69 offset:12288\n"
    "ds_read_b64_tr_b16 a[178:179], v69 offset:13312\n"
    "v_pk_add_f32 v[104:105], v[120:121], v[2:3] op_sel_hi:[1,0] neg_lo:[0,1] neg_hi:[0,1]\n"
    "v_pk_add_f32 v[106:107], v[122:123], v[2:3] op_sel_hi:[1,0] neg_lo:[0,1] neg_hi:[0,1]\n"
    "v_add_f32_dpp v4, v4, v4 row_ror:4 row_mask:0xf bank_mask:0xf\n"
    "v_pk_add_f32 v[108:109], v[124:125], v[2:3] op_sel_hi:[1,0] neg_lo:[0,1] neg_hi:[0,1]\n"
    "v_pk_add_f32 v[110:111], v[126:127], v[2:3] op_sel_hi:[1,0] neg_lo:[0,1] neg_hi:[0,1]\n"
    "s_add_u32 m0, s66, 0x2400\n"
    "s_add_u32 s61, s59, 0x208000\n"
    "buffer_load_dwordx4 v67, s[80:83], s61 offen lds\n"
    "ds_read_b64_tr_b16 a[180:181], v69 offset:45056\n"
    "ds_read_b64_tr_b16 a[182:183], v69 offset:46080\n"
    "v_add_f32_dpp v4, v4, v4 row_ror:2 row_mask:0xf bank_mask:0xf\n"
    "v_cvt_pk_bf16_f32 v96, v104, v105\n"
    "v_cvt_pk_bf16_f32 v97, v106, v107\n"
    "v_add_f32_dpp v4, v4, v4 row_ror:1 row_mask:0xf bank_mask:0xf\n"
    "v_mov_b32 v5, v4\n"
    "s_add_u32 m0, s66, 0x2800\n"
    "s_add_u32 s61, s59, 0x210000\n"
    "buffer_load_dwordx4 v68, s[80:83], s61 offen lds\n"
    "ds_read_b64_tr_b16 a[184:185], v71 offset:12288\n"
    "ds_read_b64_tr_b16 a[186:187], v71 offset:13312\n"
    "v_cvt_pk_bf16_f32 v98, v108, v109\n"
    "v_cvt_pk_bf16_f32 v99, v110, v111\n"
    "v_permlane32_swap_b32 v4, v5\n"
    "v_add_f32 v4, v4, v5\n"
    "v_mov_b32 v5, v4\n"
    "s_add_u32 m0, s66, 0x2c00\n"
    "s_add_u32 s61, s59, 0x218000\n"
    "buffer_load_dwordx4 v68, s[80:83], s61 offen lds\n"
    "ds_read_b64_tr_b16 a[188:189], v71 offset:45056\n"
    "ds_read_b64_tr_b16 a[190:191], v71 offset:46080\n"
    "v_lshlrev_b32 v104, 16, v96\n"
    "v_and_b32 v105, 0xffff0000, v96\n"
    "v_permlane16_swap_b32 v4, v5\n"
    "v_add_f32 v4, v4, v5\n"
    "v_mul_f32 v4, 0x3b000000, v4\n"
    "s_add_u32 m0, s66, 0x6000\n"
    "s_add_u32 s61, s59, 0x220000\n"
    "buffer_load_dwordx4 v67, s[80:83], s61 offen lds\n"
    "ds_read_b64_tr_b16 a[240:241], v70 offset:12288\n"
    "ds_read_b64_tr_b16 a[242:243], v70 offset:13312\n"
    "v_cvt_pk_bf16_f32 v4, v4, v4\n"
    "v_and_b32 v4, 0xffff0000, v4\n"
    "v_add_f32 v4, 0x3727c5ac, v4\n"
    "v_cvt_pk_bf16_f32 v4, v4, v4\n"
    "v_and_b32 v4, 0xffff0000, v4\n"
    "s_add_u32 m0, s66, 0x6400\n"
    "s_add_u32 s61, s59, 0x228000\n"
    "buffer_load_dwordx4 v67, s[80:83], s61 offen lds\n"
    "ds_read_b64_tr_b16 a[244:245], v70 offset:45056\n"
    "ds_read_b64_tr_b16 a[246:247], v70 offset:46080\n"
    "v_sqrt_f32 v4, v4\n"
    "v_lshlrev_b32 v106, 16, v97\n"
    "v_and_b32 v107, 0xffff0000, v97\n"
    "v_cvt_pk_bf16_f32 v4, v4, v4\n"
    "v_and_b32 v4, 0xffff0000, v4\n"
    "s_add_u32 m0, s66, 0x6800\n"
    "s_add_u32 s61, s59, 0x230000\n"
    "buffer_load_dwordx4 v68, s[80:83], s61 offen lds\n"
    "ds_read_b64_tr_b16 a[248:249], v72 offset:12288\n"
    "ds_read_b64_tr_b16 a[250:251], v72 offset:13312\n"
    "v_rcp_f32 v4, v4\n"
    "v_lshlrev_b32 v108, 16, v98\n"
    "v_and_b32 v109, 0xffff0000, v98\n"
    "v_lshlrev_b32 v110, 16, v99\n"
    "v_and_b32 v111, 0xffff0000, v99\n"
    "s_add_u32 m0, s66, 0x6c00\n"
    "s_add_u32 s61, s59, 0x238000\n"
    "buffer_load_dwordx4 v68, s[80:83], s61 offen lds\n"
    "ds_read_b64_tr_b16 a[252:253], v72 offset:45056\n"
    "ds_read_b64_tr_b16 a[254:255], v72 offset:46080\n"
    "v_pk_mul_f32 v[104:105], v[104:105], v[4:5] op_sel_hi:[1,0]\n"
    "v_pk_mul_f32 v[106:107], v[106:107], v[4:5] op_sel_hi:[1,0]\n"
    "v_pk_mul_f32 v[108:109], v[108:109], v[4:5] op_sel_hi:[1,0]\n"
    "v_pk_mul_f32 v[110:111], v[110:111], v[4:5] op_sel_hi:[1,0]\n"
    "v_cvt_pk_bf16_f32 v96, v104, v105\n"
    "s_waitcnt vmcnt(17)\n"
    "s_waitcnt lgkmcnt(0)\n"
    "s_barrier\n"
    "s_add_u32 m0, s66, 0x3000\n"
    "s_add_u32 s61, s59, 0x300000\n"
    "buffer_load_dwordx4 v67, s[80:83], s61 offen lds\n"
    "ds_read_b64_tr_b16 v[124:125], v69 offset:0\n"
    "ds_read_b64_tr_b16 v[126:127], v69 offset:1024\n"
    "v_cvt_pk_bf16_f32 v97, v106, v107\n"
    "v_cvt_pk_bf16_f32 v98, v108, v109\n"
    "v_cvt_pk_bf16_f32 v99, v110, v111\n"
    "v_min_u32 v96, -2, v96\n"
    "v_min_u32 v97, -2, v97\n"
    "s_add_u32 m0, s66, 0x3400\n"
    "s_add_u32 s61, s59, 0x308000\n"
    "buffer_load_dwordx4 v67, s[80:83], s61 offen lds\n"
    "ds_read_b64_tr_b16 v[120:121], v69 offset:32768\n"
    "ds_read_b64_tr_b16 v[122:123], v69 offset:33792\n"
    "v_min_u32 v98, -2, v98\n"
    "v_min_u32 v99, -2, v99\n"
    "buffer_store_dwordx4 v[96:99], v103, s[12:15], s58 offen sc1\n"
    "s_add_u32 m0, s66, 0x3800\n"
    "s_add_u32 s61, s59, 0x310000\n"
    "buffer_load_dwordx4 v68, s[80:83], s61 offen lds\n"
    "ds_read_b64_tr_b16 v[116:117], v71 offset:0\n"
    "ds_read_b64_tr_b16 v[118:119], v71 offset:1024\n"
    "s_add_u32 m0, s66, 0x3c00\n"
    "s_add_u32 s61, s59, 0x318000\n"
    "buffer_load_dwordx4 v68, s[80:83], s61 offen lds\n"
    "ds_read_b64_tr_b16 v[112:113], v71 offset:32768\n"
    "ds_read_b64_tr_b16 v[114:115], v71 offset:33792\n"
    "s_add_u32 m0, s66, 0x7000\n"
    "s_add_u32 s61, s59, 0x320000\n"
    "buffer_load_dwordx4 v67, s[80:83], s61 offen lds\n"
    "ds_read_b64_tr_b16 v[108:109], v70 offset:0\n"
    "ds_read_b64_tr_b16 v[110:111], v70 offset:1024\n"
    "s_add_u32 m0, s66, 0x7400\n"
    "s_add_u32 s61, s59, 0x328000\n"
    "buffer_load_dwordx4 v67, s[80:83], s61 offen lds\n"
    "ds_read_b64_tr_b16 v[104:105], v70 offset:32768\n"
    "ds_read_b64_tr_b16 v[106:107], v70 offset:33792\n"
    "s_add_u32 m0, s66, 0x7800\n"
    "s_add_u32 s61, s59, 0x330000\n"
    "buffer_load_dwordx4 v68, s[80:83], s61 offen lds\n"
    "ds_read_b64_tr_b16 v[100:101], v72 offset:0\n"
    "ds_read_b64_tr_b16 v[102:103], v72 offset:1024\n"
    "s_add_u32 m0, s66, 0x7c00\n"
    "s_add_u32 s61, s59, 0x338000\n"
    "buffer_load_dwordx4 v68, s[80:83], s61 offen lds\n"
    "ds_read_b64_tr_b16 v[96:97], v72 offset:32768\n"
    "ds_read_b64_tr_b16 v[98:99], v72 offset:33792\n"
    "s_waitcnt vmcnt(17)\n"
    "s_waitcnt lgkmcnt(0)\n"
    "s_barrier\n"
    "s_lshl_b32 s61, s42, 12\n"
    "s_add_u32 s61, s61, s67\n"
    "v_add_u32 v2, s61, v0\n"
    "v_lshlrev_b32 v2, 2, v2\n"
    "s_getpc_b64 s[80:81]\n"
    "tc_%=:\n"
    "s_mov_b32 s82, Lcode_end-tc_%=\n"
    "s_and_b32 s81, s81, 0xffff\n"
    "s_mov_b32 m0, 0x1000\n"
    "s_nop 0\n"
    "buffer_load_dword v2, s[80:83], 0 offen lds\n"
    "ds_read_b64_tr_b16 v[92:93], v69 offset:4096\n"
    "ds_read_b64_tr_b16 v[94:95], v69 offset:5120\n"
    "ds_write_b128 v66, v[124:127]\n"
    "ds_read_b64_tr_b16 v[88:89], v69 offset:36864\n"
    "ds_read_b64_tr_b16 v[90:91], v69 offset:37888\n"
    "ds_write_b128 v66, v[120:123] offset:1024\n"
    "ds_read_b64_tr_b16 v[124:125], v71 offset:4096\n"
    "ds_read_b64_tr_b16 v[126:127], v71 offset:5120\n"
    "ds_write_b128 v66, v[116:119] offset:2048\n"
    "ds_read_b64_tr_b16 v[120:121], v71 offset:36864\n"
    "ds_read_b64_tr_b16 v[122:123], v71 offset:37888\n"
    "ds_write_b128 v66, v[112:115] offset:3072\n"
    "ds_read_b64_tr_b16 v[116:117], v70 offset:4096\n"
    "ds_read_b64_tr_b16 v[118:119], v70 offset:5120\n"
    "ds_write_b128 v66, v[108:111] offset:16384\n"
    "ds_read_b64_tr_b16 v[112:113], v70 offset:36864\n"
    "ds_read_b64_tr_b16 v[114:115], v70 offset:37888\n"
    "ds_write_b128 v66, v[104:107] offset:17408\n"
    "ds_read_b64_tr_b16 v[108:109], v72 offset:4096\n"
    "ds_read_b64_tr_b16 v[110:111], v72 offset:5120\n"
    "ds_write_b128 v66, v[100:103] offset:18432\n"
    "ds_read_b64_tr_b16 v[104:105], v72 offset:36864\n"
    "ds_read_b64_tr_b16 v[106:107], v72 offset:37888\n"
    "ds_write_b128 v66, v[96:99] offset:19456\n"
    "s_waitcnt vmcnt(10)\n"
    "s_waitcnt lgkmcnt(0)\n"
    "s_barrier\n"
    "v_mov_b64 v[100:101], -1\n"
    "v_mov_b64 v[102:103], -1\n"
    "buffer_store_dwordx4 v[100:103], v0, s[8:11], s50 offen sc1\n"
    "buffer_store_dwordx4 v[100:103], v0, s[8:11], s50 offen offset:1024 sc1\n"
    "ds_read_b64_tr_b16 v[96:97], v69 offset:8192\n"
    "ds_read_b64_tr_b16 v[98:99], v69 offset:9216\n"
    "ds_write_b128 v66, v[92:95] offset:4096\n"
    "ds_read_b64_tr_b16 v[100:101], v69 offset:40960\n"
    "ds_read_b64_tr_b16 v[102:103], v69 offset:41984\n"
    "ds_write_b128 v66, v[88:91] offset:5120\n"
    "ds_read_b64_tr_b16 v[92:93], v71 offset:8192\n"
    "ds_read_b64_tr_b16 v[94:95], v71 offset:9216\n"
    "ds_write_b128 v66, v[124:127] offset:6144\n"
    "ds_read_b64_tr_b16 v[88:89], v71 offset:40960\n"
    "ds_read_b64_tr_b16 v[90:91], v71 offset:41984\n"
    "ds_write_b128 v66, v[120:123] offset:7168\n"
    "ds_read_b64_tr_b16 v[124:125], v70 offset:8192\n"
    "ds_read_b64_tr_b16 v[126:127], v70 offset:9216\n"
    "ds_write_b128 v66, v[116:119] offset:20480\n"
    "ds_read_b64_tr_b16 v[120:121], v70 offset:40960\n"
    "ds_read_b64_tr_b16 v[122:123], v70 offset:41984\n"
    "ds_write_b128 v66, v[112:115] offset:21504\n"
    "ds_read_b64_tr_b16 v[116:117], v72 offset:8192\n"
    "ds_read_b64_tr_b16 v[118:119], v72 offset:9216\n"
    "ds_write_b128 v66, v[108:111] offset:22528\n"
    "ds_read_b64_tr_b16 v[112:113], v72 offset:40960\n"
    "ds_read_b64_tr_b16 v[114:115], v72 offset:41984\n"
    "ds_write_b128 v66, v[104:107] offset:23552\n"
    "s_waitcnt vmcnt(3)\n"
    "s_waitcnt lgkmcnt(0)\n"
    "s_barrier\n"
    "ds_read_b64_tr_b16 v[108:109], v69 offset:12288\n"
    "ds_read_b64_tr_b16 v[110:111], v69 offset:13312\n"
    "ds_write_b128 v66, v[96:99] offset:8192\n"
    "ds_read_b64_tr_b16 v[104:105], v69 offset:45056\n"
    "ds_read_b64_tr_b16 v[106:107], v69 offset:46080\n"
    "ds_write_b128 v66, v[100:103] offset:9216\n"
    "ds_read_b64_tr_b16 v[96:97], v71 offset:12288\n"
    "ds_read_b64_tr_b16 v[98:99], v71 offset:13312\n"
    "ds_write_b128 v66, v[92:95] offset:10240\n"
    "ds_read_b64_tr_b16 v[100:101], v71 offset:45056\n"
    "ds_read_b64_tr_b16 v[102:103], v71 offset:46080\n"
    "ds_write_b128 v66, v[88:91] offset:11264\n"
    "ds_read_b64_tr_b16 v[92:93], v70 offset:12288\n"
    "ds_read_b64_tr_b16 v[94:95], v70 offset:13312\n"
    "ds_write_b128 v66, v[124:127] offset:24576\n"
    "ds_read_b64_tr_b16 v[88:89], v70 offset:45056\n"
    "ds_read_b64_tr_b16 v[90:91], v70 offset:46080\n"
    "ds_write_b128 v66, v[120:123] offset:25600\n"
    "ds_read_b64_tr_b16 v[124:125], v72 offset:12288\n"
    "ds_read_b64_tr_b16 v[126:127], v72 offset:13312\n"
    "ds_write_b128 v66, v[116:119] offset:26624\n"
    "ds_read_b64_tr_b16 v[120:121], v72 offset:45056\n"
    "ds_read_b64_tr_b16 v[122:123], v72 offset:46080\n"
    "ds_write_b128 v66, v[112:115] offset:27648\n"
    "s_waitcnt lgkmcnt(0)\n"
    "s_barrier\n"
    "ds_write_b128 v66, v[108:111] offset:12288\n"
    "ds_write_b128 v66, v[104:107] offset:13312\n"
    "ds_write_b128 v66, v[96:99] offset:14336\n"
    "ds_write_b128 v66, v[100:103] offset:15360\n"
    "ds_write_b128 v66, v[92:95] offset:28672\n"
    "ds_write_b128 v66, v[88:91] offset:29696\n"
    "ds_write_b128 v66, v[124:127] offset:30720\n"
    "ds_write_b128 v66, v[120:123] offset:31744\n"
    "s_add_u32 s58, s58, 0x20000\n"
    "s_add_u32 s57, s57, 0x40000\n"
    "s_cmp_eq_u32 s41, 2\n"
    "s_cselect_b32 s57, 0x200000, s57\n"
    "s_lshr_b32 s99, s41, 1\n"
    "s_or_b32 s99, s99, s40\n"
    "s_mov_b32 s72, 0xfff0ffff\n"
    "s_mov_b32 s73, 0xf000ff00\n"
    "s_mov_b32 s74, 0xffe0fffe\n"
    "s_mov_b32 s75, 0xe000fe00\n"
    "s_mov_b32 s76, 0xffc0fffc\n"
    "s_mov_b32 s77, 0xc000fc00\n"
    "s_mov_b32 s78, 0xff80fff8\n"
    "s_mov_b32 s79, 0x8000f800\n"
    "s_waitcnt vmcnt(0) lgkmcnt(0)\n"
    "s_nop 7\n"
    "s_barrier\n"
    "s_mov_b32 s69, 0\n"
    "s_mov_b32 s33, 1\n"
    "s_branch entry_%=\n"
    "turn_%=:\n"
    "s_mul_i32 s14, s40, 0x3400000\n"
    "buffer_load_dwordx2 v[4:5], off, s[12:15], s70 sc1\n"
    "buffer_load_dwordx4 v[124:127], off, s[8:11], s51 sc1\n"
    "v_add_u32 v2, s66, v0\n"
    "ds_read_b128 v[116:119], v0 offset:28736\n"
    "ds_read_b128 v[92:95], v0 offset:24640\n"
    "ds_read_b128 v[80:83], v0 offset:24640\n"
    "ds_read_b128 v[76:79], v0 offset:24640\n"
    "ds_read_b128 v[88:91], v0 offset:24640\n"
    "ds_read_b128 v[64:67], v0 offset:24640\n"
    "ds_read_b128 v[8:11], v2\n"
    "ds_read_b128 v[12:15], v2 offset:1024\n"
    "ds_read_b128 v[100:103], v2 offset:16384\n"
    "ds_read_b128 v[104:107], v2 offset:17408\n"
    "ds_read_b128 v[24:27], v2 offset:2048\n"
    "ds_read_b128 v[28:31], v2 offset:3072\n"
    "ds_read_b128 v[16:19], v2 offset:18432\n"
    "ds_read_b128 v[20:23], v2 offset:19456\n"
    "s_waitcnt lgkmcnt(13)\n"
    "v_add_u32 v116, s68, v116\n"
    "s_waitcnt vmcnt(1)\n"
    "v_readfirstlane_b32 s61, v4\n"
    "v_readfirstlane_b32 s62, v5\n"
    "s_sub_u32 s61, s61, 1\n"
    "s_sub_u32 s62, s62, s1\n"
    "s_max_i32 s62, s62, 0\n"
    "s_cmp_eq_u32 s61, s42\n"
    "s_cselect_b32 s62, s62, 0\n"
    "s_add_u32 s1, s1, s62\n"
    "s_cmp_eq_u32 s42, 0\n"
    "s_cbranch_scc1 hufirst_%=\n"
    "hupoll_%=:\n"
    "s_waitcnt vmcnt(0)\n"
    "v_cmp_ne_u32 vcc, s42, v124\n"
    "v_readfirstlane_b32 s43, v125\n"
    "v_readfirstlane_b32 s61, v126\n"
    "v_readfirstlane_b32 s62, v127\n"
    "s_cbranch_vccnz huagain_%=\n"
    "s_cmp_eq_u32 s61, s42\n"
    "s_cselect_b32 s62, s62, s43\n"
    "s_sub_u32 s62, s62, s43\n"
    "s_cmp_gt_i32 s62, 33\n"
    "s_cselect_b32 s62, s62, 0\n"
    "s_add_u32 s43, s43, s62\n"
    "s_add_u32 s43, s43, s97\n"
    "s_sub_u32 s61, s1, s43\n"
    "s_max_i32 s61, s61, 0\n"
    "s_add_u32 s43, s43, s61\n"
    "hugot_%=:\n"
    "s_mov_b32 s63, 0\n"
    "huok_%=:\n"
    "s_memrealtime s[80:81]\n"
    "s_waitcnt lgkmcnt(0)\n"
    "s_sub_u32 s61, s43, s80\n"
    "s_cmp_gt_u32 s61, 0xbb8\n"
    "s_cbranch_scc1 hulate_%=\n"
    "s_cmp_gt_u32 s61, 12\n"
    "s_cbranch_scc1 huok_%=\n"
    "s_sub_u32 s61, 15, s61\n"
    "s_lshl_b32 s61, s61, 2\n"
    "s_getpc_b64 s[82:83]\n"
    "s_add_u32 s82, s82, s61\n"
    "s_addc_u32 s83, s83, 0\n"
    "s_setpc_b64 s[82:83]\n"
    "s_nop 5\n"
    "s_nop 4\n"
    "s_nop 4\n"
    "s_nop 5\n"
    "s_nop 4\n"
    "s_nop 4\n"
    "s_nop 5\n"
    "s_nop 4\n"
    "s_nop 4\n"
    "s_nop 5\n"
    "s_nop 4\n"
    "s_nop 4\n"
    "cload_%=:\n"
    "buffer_load_dwordx4 v[108:111], v0, s[8:11], s46 offen sc1\n"
    "s_nop 9\n"
    "buffer_load_dwordx4 v[72:75], v0, s[8:11], s46 offen offset:1024 sc1\n"
    "s_add_u32 s61, s42, 1\n"
    "s_add_u32 s62, s43, s71\n"
    "s_add_u32 s62, s62, s63\n"
    "s_mov_b32 s14, s2\n"
    "s_nop 15\n"
    "s_nop 7\n"
    "buffer_load_dwordx4 v[96:99], v0, s[8:11], s46 offen offset:2048 sc1\n"
    "s_nop 9\n"
    "buffer_load_dwordx4 v[68:71], v0, s[8:11], s46 offen offset:3072 sc1\n"
    "s_nop 15\n"
    "s_nop 7\n"
    "buffer_load_dwordx4 v[84:87], v0, s[8:11], s34 offen sc1\n"
    "s_nop 9\n"
    "buffer_load_dwordx4 v[124:127], v0, s[8:11], s34 offen offset:1024 sc1\n"
    "s_nop 15\n"
    "buffer_load_dwordx4 v[112:115], v0, s[8:11], s34 offen offset:2048 sc1\n"
    "s_waitcnt lgkmcnt(0)\n"
    "s_waitcnt vmcnt(6)\n"
    "v_smfmac_f32_16x16x64_bf16 v[76:79], v[108:111], v[8:15], v1\n"
    "ds_read_b128 v[8:11], v2 offset:4096\n"
    "ds_read_b128 v[12:15], v2 offset:5120\n"
    "v_smfmac_f32_16x16x64_bf16 v[88:91], v[108:111], v[100:107], v1\n"
    "ds_read_b128 v[100:103], v2 offset:20480\n"
    "ds_read_b128 v[104:107], v2 offset:21504\n"
    "v_smfmac_f32_16x16x64_bf16 v[92:95], v[108:111], a[0:7], v1\n"
    "buffer_load_dwordx4 v[120:123], v0, s[8:11], s34 offen offset:3072 sc1\n"
    "v_smfmac_f32_16x16x64_bf16 v[80:83], v[108:111], a[64:71], v1\n"
    "s_waitcnt vmcnt(6)\n"
    "v_smfmac_f32_16x16x64_bf16 v[76:79], v[72:75], v[24:31], v1\n"
    "ds_read_b128 v[24:27], v2 offset:6144\n"
    "ds_read_b128 v[28:31], v2 offset:7168\n"
    "v_smfmac_f32_16x16x64_bf16 v[88:91], v[72:75], v[16:23], v1\n"
    "ds_read_b128 v[16:19], v2 offset:22528\n"
    "ds_read_b128 v[20:23], v2 offset:23552\n"
    "v_smfmac_f32_16x16x64_bf16 v[92:95], v[72:75], a[8:15], v1\n"
    "v_smfmac_f32_16x16x64_bf16 v[80:83], v[72:75], a[72:79], v1\n"
    "v_smfmac_f32_16x16x64_bf16 v[64:67], v[108:111], a[128:135], v1\n"
    "v_smfmac_f32_16x16x64_bf16 v[64:67], v[72:75], a[136:143], v1\n"
    "s_waitcnt vmcnt(5) lgkmcnt(6)\n"
    "v_smfmac_f32_16x16x64_bf16 v[76:79], v[96:99], v[8:15], v1\n"
    "ds_read_b128 v[8:11], v2 offset:8192\n"
    "ds_read_b128 v[12:15], v2 offset:9216\n"
    "s_waitcnt lgkmcnt(6)\n"
    "v_smfmac_f32_16x16x64_bf16 v[88:91], v[96:99], v[100:107], v1\n"
    "ds_read_b128 v[100:103], v2 offset:24576\n"
    "ds_read_b128 v[104:107], v2 offset:25600\n"
    "v_smfmac_f32_16x16x64_bf16 v[92:95], v[96:99], a[16:23], v1\n"
    "v_smfmac_f32_16x16x64_bf16 v[80:83], v[96:99], a[80:87], v1\n"
    "s_waitcnt vmcnt(4) lgkmcnt(6)\n"
    "v_smfmac_f32_16x16x64_bf16 v[76:79], v[68:71], v[24:31], v1\n"
    "ds_read_b128 v[24:27], v2 offset:10240\n"
    "ds_read_b128 v[28:31], v2 offset:11264\n"
    "s_waitcnt lgkmcnt(6)\n"
    "v_smfmac_f32_16x16x64_bf16 v[88:91], v[68:71], v[16:23], v1\n"
    "ds_read_b128 v[16:19], v2 offset:26624\n"
    "ds_read_b128 v[20:23], v2 offset:27648\n"
    "v_smfmac_f32_16x16x64_bf16 v[92:95], v[68:71], a[24:31], v1\n"
    "v_smfmac_f32_16x16x64_bf16 v[80:83], v[68:71], a[88:95], v1\n"
    "v_smfmac_f32_16x16x64_bf16 v[64:67], v[96:99], a[144:151], v1\n"
    "s_waitcnt vmcnt(3) lgkmcnt(6)\n"
    "v_smfmac_f32_16x16x64_bf16 v[76:79], v[84:87], v[8:15], v1\n"
    "ds_read_b128 v[8:11], v2 offset:12288\n"
    "ds_read_b128 v[12:15], v2 offset:13312\n"
    "s_waitcnt lgkmcnt(6)\n"
    "v_smfmac_f32_16x16x64_bf16 v[88:91], v[84:87], v[100:107], v1\n"
    "ds_read_b128 v[100:103], v2 offset:28672\n"
    "ds_read_b128 v[104:107], v2 offset:29696\n"
    "v_smfmac_f32_16x16x64_bf16 v[92:95], v[84:87], a[32:39], v1\n"
    "v_smfmac_f32_16x16x64_bf16 v[80:83], v[84:87], a[96:103], v1\n"
    "s_waitcnt vmcnt(2) lgkmcnt(6)\n"
    "v_smfmac_f32_16x16x64_bf16 v[76:79], v[124:127], v[24:31], v1\n"
    "ds_read_b128 v[24:27], v2 offset:14336\n"
    "ds_read_b128 v[28:31], v2 offset:15360\n"
    "s_waitcnt lgkmcnt(6)\n"
    "v_smfmac_f32_16x16x64_bf16 v[88:91], v[124:127], v[16:23], v1\n"
    "ds_read_b128 v[16:19], v2 offset:30720\n"
    "ds_read_b128 v[20:23], v2 offset:31744\n"
    "v_smfmac_f32_16x16x64_bf16 v[92:95], v[124:127], a[40:47], v1\n"
    "v_smfmac_f32_16x16x64_bf16 v[80:83], v[124:127], a[104:111], v1\n"
    "s_waitcnt vmcnt(1) lgkmcnt(6)\n"
    "v_smfmac_f32_16x16x64_bf16 v[76:79], v[112:115], v[8:15], v1\n"
    "s_waitcnt lgkmcnt(4)\n"
    "v_mov_b64 v[4:5], 0\n"
    "v_smfmac_f32_16x16x64_bf16 v[88:91], v[112:115], v[100:107], v1\n"
    "v_mov_b64 v[6:7], 0\n"
    "v_smfmac_f32_16x16x64_bf16 v[92:95], v[112:115], a[48:55], v1\n"
    "v_lshlrev_b32 v3, 20, v0\n"
    "v_smfmac_f32_16x16x64_bf16 v[80:83], v[112:115], a[112:119], v1\n"
    "s_waitcnt vmcnt(0) lgkmcnt(2)\n"
    "v_and_b32 v3, 0x20000000, v3\n"
    "v_smfmac_f32_16x16x64_bf16 v[76:79], v[120:123], v[24:31], v1\n"
    "ds_read_b128 v[104:107], v118 offset:8192\n"
    "s_waitcnt lgkmcnt(1)\n"
    "v_lshl_or_b32 v3, v0, 3, v3\n"
    "v_smfmac_f32_16x16x64_bf16 v[88:91], v[120:123], v[16:23], v1\n"
    "ds_read_b128 v[100:103], v118 offset:9216\n"
    "v_mov_b32 v8, s61\n"
    "v_smfmac_f32_16x16x64_bf16 v[92:95], v[120:123], a[56:63], v1\n"
    "ds_read_b128 v[28:31], v118 offset:10240\n"
    "v_add_f32 v76, v76, v78\n"
    "v_smfmac_f32_16x16x64_bf16 v[80:83], v[120:123], a[120:127], v1\n"
    "ds_read_b128 v[24:27], v118 offset:11264\n"
    "v_add_f32 v77, v77, v79\n"
    "v_smfmac_f32_16x16x64_bf16 v[4:7], v[108:111], a[192:199], v1\n"
    "ds_read_b128 v[20:23], v118 offset:8256\n"
    "v_add_f32 v88, v88, v90\n"
    "v_add_f32 v89, v89, v91\n"
    "v_cvt_pk_bf16_f32 v78, v76, v88\n"
    "v_cvt_pk_bf16_f32 v79, v77, v89\n"
    "v_smfmac_f32_16x16x64_bf16 v[64:67], v[68:71], a[152:159], v1\n"
    "ds_write2_b32 v116, v78, v79 offset0:144 offset1:216\n"
    "v_smfmac_f32_16x16x64_bf16 v[4:7], v[72:75], a[200:207], v1\n"
    "ds_read_b128 v[88:91], v118 offset:9280\n"
    "v_add_f32 v92, v92, v94\n"
    "v_add_f32 v93, v93, v95\n"
    "v_add_f32 v80, v80, v82\n"
    "v_smfmac_f32_16x16x64_bf16 v[64:67], v[84:87], a[160:167], v1\n"
    "ds_read_b128 v[76:79], v118 offset:10304\n"
    "v_add_f32 v81, v81, v83\n"
    "v_cvt_pk_bf16_f32 v94, v92, v80\n"
    "v_cvt_pk_bf16_f32 v95, v93, v81\n"
    "v_smfmac_f32_16x16x64_bf16 v[64:67], v[124:127], a[168:175], v1\n"
    "ds_write2_b32 v116, v94, v95 offset0:0 offset1:72\n"
    "v_smfmac_f32_16x16x64_bf16 v[4:7], v[96:99], a[208:215], v1\n"
    "ds_read_b128 v[80:83], v118 offset:11328\n"
    "v_cmp_u_f32 vcc, v92, v93\n"
    "v_smfmac_f32_16x16x64_bf16 v[64:67], v[112:115], a[176:183], v1\n"
    "v_mov_b32 v9, s62\n"
    "v_smfmac_f32_16x16x64_bf16 v[64:67], v[120:123], a[184:191], v1\n"
    "s_cbranch_vccnz slowI_%=\n"
    "cok_%=:\n"
    "v_smfmac_f32_16x16x64_bf16 v[4:7], v[68:71], a[216:223], v1\n"
    "s_waitcnt lgkmcnt(1)\n"
    "s_barrier\n"
    "v_smfmac_f32_16x16x64_bf16 v[4:7], v[84:87], a[224:231], v1\n"
    "ds_read_b128 v[108:111], v117\n"
    "ds_read_b128 v[96:99], v117 offset:64\n"
    "v_smfmac_f32_16x16x64_bf16 v[4:7], v[124:127], a[232:239], v1\n"
    "ds_read_b128 v[92:95], v117 offset:128\n"
    "ds_read_b128 v[84:87], v117 offset:192\n"
    "v_smfmac_f32_16x16x64_bf16 v[4:7], v[112:115], a[240:247], v1\n"
    "ds_read_b128 v[124:127], v117 offset:576\n"
    "v_add_u32 v2, 0x240, v116\n"
    "v_smfmac_f32_16x16x64_bf16 v[4:7], v[120:123], a[248:255], v1\n"
    "v_add_f32 v64, v64, v66\n"
    "s_waitcnt lgkmcnt(4)\n"
    "v_mfma_f32_16x16x32_bf16 v[112:115], v[104:107], v[108:111], 0\n"
    "s_waitcnt lgkmcnt(3)\n"
    "v_add_f32 v65, v65, v67\n"
    "v_mfma_f32_16x16x32_bf16 v[112:115], v[100:103], v[96:99], v[112:115]\n"
    "s_waitcnt lgkmcnt(2)\n"
    "v_add_f32 v4, v4, v6\n"
    "v_add_f32 v5, v5, v7\n"
    "v_mfma_f32_16x16x32_bf16 v[112:115], v[28:31], v[92:95], v[112:115]\n"
    "s_waitcnt lgkmcnt(1)\n"
    "v_cvt_pk_bf16_f32 v66, v64, v4\n"
    "v_cvt_pk_bf16_f32 v67, v65, v5\n"
    "v_mfma_f32_16x16x32_bf16 v[112:115], v[24:27], v[84:87], v[112:115]\n"
    "ds_write2_b32 v2, v66, v67 offset0:144 offset1:216\n"
    "v_mfma_f32_16x16x32_bf16 v[120:123], v[20:23], v[108:111], 0\n"
    "ds_read_b128 v[104:107], v117 offset:640\n"
    "ds_read_b128 v[100:103], v117 offset:704\n"
    "v_mfma_f32_16x16x32_bf16 v[120:123], v[88:91], v[96:99], v[120:123]\n"
    "ds_read_b128 v[72:75], v117 offset:768\n"
    "ds_read_b128 v[68:71], v0 offset:24640\n"
    "s_mov_b32 vcc_lo, 0xcccccccc\n"
    "v_mfma_f32_16x16x32_bf16 v[120:123], v[76:79], v[92:95], v[120:123]\n"
    "s_mov_b32 vcc_hi, vcc_lo\n"
    "v_max3_f32 v4, v112, v113, v114\n"
    "v_mfma_f32_16x16x32_bf16 v[120:123], v[80:83], v[84:87], v[120:123]\n"
    "s_waitcnt lgkmcnt(4)\n"
    "s_barrier\n"
    "v_mfma_f32_16x16x32_bf16 v[76:79], v[124:127], v[108:111], 0\n"
    "ds_read_b128 v[88:91], v117 offset:1152\n"
    "ds_read_b128 v[80:83], v117 offset:1216\n"
    "s_waitcnt lgkmcnt(5)\n"
    "v_mfma_f32_16x16x32_bf16 v[76:79], v[104:107], v[96:99], v[76:79]\n"
    "ds_read_b128 v[108:111], v117 offset:1280\n"
    "ds_read_b128 v[64:67], v117 offset:1344\n"
    "s_waitcnt lgkmcnt(6)\n"
    "v_mfma_f32_16x16x32_bf16 v[76:79], v[100:103], v[92:95], v[76:79]\n"
    "s_waitcnt lgkmcnt(5)\n"
    "v_max3_f32 v5, v115, v120, v121\n"
    "v_max3_f32 v4, v4, v122, v123\n"
    "v_mfma_f32_16x16x32_bf16 v[76:79], v[72:75], v[84:87], v[76:79]\n"
    "v_max_f32 v4, v4, v5\n"
    "v_cndmask_b32 v112, v112, v120, vcc\n"
    "s_waitcnt lgkmcnt(3)\n"
    "v_smfmac_f32_16x16x64_bf16 v[68:71], v[88:91], v[128:135], v1\n"
    "buffer_store_dwordx2 v[8:9], v3, s[16:19], s52 offen sc1\n"
    "s_nop 1\n"
    "v_max3_f32 v5, v4, v76, v77\n"
    "v_max3_f32 v2, v4, v76, v77\n"
    "s_waitcnt lgkmcnt(2)\n"
    "v_smfmac_f32_16x16x64_bf16 v[68:71], v[80:83], v[136:143], v1\n"
    "ds_read_b128 v[92:95], v0 offset:25664\n"
    "v_permlane32_swap_b32 v5, v2\n"
    "v_max_f32 v4, v5, v2\n"
    "v_max_f32 v5, v5, v2\n"
    "v_cndmask_b32 v113, v113, v121, vcc\n"
    "v_cndmask_b32 v114, v114, v122, vcc\n"
    "v_permlane16_swap_b32 v4, v5\n"
    "v_max_f32 v4, v4, v5\n"
    "v_mul_f32 v4, s32, v4\n"
    "v_cndmask_b32 v115, v115, v123, vcc\n"
    "v_pk_fma_f32 v[76:77], v[76:77], s[32:33], v[4:5] op_sel_hi:[1,0,0] neg_lo:[0,0,1] neg_hi:[0,0,1]\n"
    "v_pk_fma_f32 v[112:113], v[112:113], s[32:33], v[4:5] op_sel_hi:[1,0,0] neg_lo:[0,0,1] neg_hi:[0,0,1]\n"
    "v_pk_fma_f32 v[114:115], v[114:115], s[32:33], v[4:5] op_sel_hi:[1,0,0] neg_lo:[0,0,1] neg_hi:[0,0,1]\n"
    "s_waitcnt lgkmcnt(2)\n"
    "v_smfmac_f32_16x16x64_bf16 v[68:71], v[108:111], v[144:151], v1\n"
    "ds_read_b128 v[104:107], v0 offset:24640\n"
    "s_waitcnt lgkmcnt(2)\n"
    "v_exp_f32 v76, v76\n"
    "v_smfmac_f32_16x16x64_bf16 v[68:71], v[64:67], v[152:159], v1\n"
    "ds_read_b128 v[100:103], v0 offset:24640\n"
    "v_exp_f32 v77, v77\n"
    "v_exp_f32 v112, v112\n"
    "v_cvt_pk_bf16_f32 v2, v76, v77\n"
    "v_exp_f32 v113, v113\n"
    "v_cndmask_b32 v3, 0, v2, vcc\n"
    "v_cvt_pk_bf16_f32 v124, v68, v69\n"
    "v_xor_b32 v2, v2, v3\n"
    "v_cvt_pk_bf16_f32 v125, v70, v71\n"
    "v_exp_f32 v114, v114\n"
    "v_mfma_f32_16x16x16_bf16 v[120:123], v[2:3], v[124:125], 0\n"
    "v_exp_f32 v115, v115\n"
    "v_cvt_pk_bf16_f32 v4, v112, v113\n"
    "s_waitcnt lgkmcnt(2)\n"
    "v_mfma_f32_16x16x16_bf16 v[84:87], v[2:3], v[94:95], 0\n"
    "v_cvt_pk_bf16_f32 v5, v114, v115\n"
    "v_mov_b32_dpp v96, v4 quad_perm:[0,1,0,1] row_mask:0xf bank_mask:0xf\n"
    "v_mov_b32_dpp v98, v4 quad_perm:[2,3,2,3] row_mask:0xf bank_mask:0xf\n"
    "v_mfma_f32_16x16x16_bf16 v[84:87], v[4:5], v[92:93], v[84:87]\n"
    "v_mov_b32_dpp v97, v5 quad_perm:[0,1,0,1] row_mask:0xf bank_mask:0xf\n"
    "v_mov_b32_dpp v99, v5 quad_perm:[2,3,2,3] row_mask:0xf bank_mask:0xf\n"
    "s_nop 1\n"
    "v_smfmac_f32_16x16x64_bf16 v[120:123], v[96:99], v[56:63], v1\n"
    "ds_read_b128 v[92:95], v0 offset:24640\n"
    "v_add_f32 v84, v84, v86\n"
    "v_add_f32 v85, v85, v87\n"
    "v_rcp_f32 v84, v84\n"
    "v_rcp_f32 v85, v85\n"
    "s_add_u32 s63, s48, 0x10000\n"
    "v_pk_mul_f32 v[120:121], v[120:121], v[84:85]\n"
    "v_pk_mul_f32 v[122:123], v[122:123], v[84:85]\n"
    "v_cvt_pk_bf16_f32 v120, v120, v122\n"
    "v_cvt_pk_bf16_f32 v121, v121, v123\n"
    "s_waitcnt lgkmcnt(2)\n"
    "v_smfmac_f32_16x16x64_bf16 v[104:107], v[88:91], v[160:167], v1\n"
    "buffer_store_dwordx2 v[120:121], v119, s[8:11], s48 offen sc1\n"
    "v_smfmac_f32_16x16x64_bf16 v[104:107], v[80:83], v[168:175], v1\n"
    "s_memrealtime s[80:81]\n"
    "s_add_u32 s61, s43, s37\n"
    "v_mov_b64 v[72:73], 0\n"
    "v_smfmac_f32_16x16x64_bf16 v[104:107], v[108:111], v[176:183], v1\n"
    "s_mov_b32 s64, 0\n"
    "v_mov_b64 v[74:75], 0\n"
    "v_smfmac_f32_16x16x64_bf16 v[104:107], v[64:67], v[184:191], v1\n"
    "s_waitcnt lgkmcnt(1)\n"
    "v_mov_b64 v[28:29], 0\n"
    "v_smfmac_f32_16x16x64_bf16 v[100:103], v[96:99], v[48:55], v1\n"
    "s_waitcnt lgkmcnt(0)\n"
    "s_sub_u32 s0, s61, s80\n"
    "v_mov_b64 v[30:31], 0\n"
    "v_smfmac_f32_16x16x64_bf16 v[92:95], v[88:91], v[192:199], v1\n"
    "ds_read_b128 v[120:123], v0 offset:26688\n"
    "v_cvt_pk_bf16_f32 v126, v104, v105\n"
    "v_cvt_pk_bf16_f32 v127, v106, v107\n"
    "v_mov_b64 v[24:25], 0\n"
    "v_mov_b64 v[26:27], 0\n"
    "v_mfma_f32_16x16x16_bf16 v[100:103], v[2:3], v[126:127], v[100:103]\n"
    "s_max_i32 s61, s0, 0\n"
    "s_min_i32 s61, s61, 24\n"
    "s_lshl_b32 s62, s61, 4\n"
    "v_smfmac_f32_16x16x64_bf16 v[92:95], v[80:83], v[200:207], v1\n"
    "s_sub_u32 s82, s38, s62\n"
    "s_subb_u32 s83, s39, 0\n"
    "s_nop 0\n"
    "v_mul_f32 v100, v100, v84\n"
    "v_mul_f32 v102, v102, v84\n"
    "v_mul_f32 v101, v101, v85\n"
    "v_mul_f32 v103, v103, v85\n"
    "v_cvt_pk_bf16_f32 v100, v100, v102\n"
    "v_cvt_pk_bf16_f32 v101, v101, v103\n"
    "v_smfmac_f32_16x16x64_bf16 v[92:95], v[108:111], v[208:215], v1\n"
    "buffer_store_dwordx2 v[100:101], v119, s[8:11], s63 offen sc1\n"
    "v_smfmac_f32_16x16x64_bf16 v[92:95], v[64:67], v[216:223], v1\n"
    "s_add_u32 s63, s48, 0x20000\n"
    "ds_read_b128 v[76:79], v0 offset:29760\n"
    "v_smfmac_f32_16x16x64_bf16 v[72:75], v[96:99], v[40:47], v1\n"
    "v_smfmac_f32_16x16x64_bf16 v[28:31], v[88:91], v[224:231], v1\n"
    "v_cvt_pk_bf16_f32 v112, v92, v93\n"
    "v_cvt_pk_bf16_f32 v113, v94, v95\n"
    "s_nop 1\n"
    "v_mfma_f32_16x16x16_bf16 v[72:75], v[2:3], v[112:113], v[72:75]\n"
    "v_smfmac_f32_16x16x64_bf16 v[28:31], v[80:83], v[232:239], v1\n"
    "s_nop 2\n"
    "v_mul_f32 v72, v72, v84\n"
    "v_mul_f32 v74, v74, v84\n"
    "v_mul_f32 v73, v73, v85\n"
    "v_mul_f32 v75, v75, v85\n"
    "v_cvt_pk_bf16_f32 v72, v72, v74\n"
    "v_cvt_pk_bf16_f32 v73, v73, v75\n"
    "v_smfmac_f32_16x16x64_bf16 v[28:31], v[108:111], v[240:247], v1\n"
    "buffer_store_dwordx2 v[72:73], v119, s[8:11], s63 offen sc1\n"
    "v_smfmac_f32_16x16x64_bf16 v[28:31], v[64:67], v[248:255], v1\n"
    "s_add_u32 s63, s48, 0x30000\n"
    "s_and_b32 s62, s61, s2\n"
    "s_cbranch_scc1 wait0_%=\n"
    "go0_%=:\n"
    "v_smfmac_f32_16x16x64_bf16 v[24:27], v[96:99], v[32:39], v1\n"
    "buffer_load_dwordx4 v[108:111], v0, s[12:15], s50 offen sc1\n"
    "ds_read_b128 v[20:23], v0\n"
    "ds_read_b128 v[68:71], v0 offset:8192\n"
    "s_nop 0\n"
    "v_cvt_pk_bf16_f32 v114, v28, v29\n"
    "v_cvt_pk_bf16_f32 v115, v30, v31\n"
    "s_nop 1\n"
    "v_mfma_f32_16x16x16_bf16 v[24:27], v[2:3], v[114:115], v[24:27]\n"
    "buffer_load_dwordx4 v[104:107], v0, s[12:15], s50 offen offset:1024 sc1\n"
    "ds_read_b128 v[100:103], v0 offset:4096\n"
    "ds_read_b128 v[28:31], v0 offset:12352\n"
    "ds_read_b128 v[96:99], v117 offset:576\n"
    "s_nop 2\n"
    "v_pk_mul_f32 v[24:25], v[24:25], v[84:85]\n"
    "v_pk_mul_f32 v[26:27], v[26:27], v[84:85]\n"
    "v_cvt_pk_bf16_f32 v24, v24, v26\n"
    "v_cvt_pk_bf16_f32 v25, v25, v27\n"
    "s_waitcnt lgkmcnt(3)\n"
    "v_mfma_f32_16x16x32_bf16 v[92:95], v[68:71], v[20:23], 0\n"
    "buffer_store_dwordx2 v[24:25], v119, s[8:11], s63 offen sc1\n"
    "s_setpc_b64 s[82:83]\n"
    "s_nop 1\n"
    "v_mov_b32 v0, v0\n"
    "s_nop 0\n"
    "v_mov_b32 v0, v0\n"
    "tail0_%=:\n"
    "s_cbranch_execz dryret_%=\n"
    "ds_read_b128 v[88:91], v0 offset:1024\n"
    "v_swap_b32 v57, v58\n"
    "s_waitcnt lgkmcnt(3)\n"
    "v_mfma_f32_16x16x32_bf16 v[84:87], v[68:71], v[100:103], 0\n"
    "ds_read_b128 v[80:83], v0 offset:9216\n"
    "v_swap_b32 v58, v60\n"
    "s_waitcnt lgkmcnt(3)\n"
    "v_mfma_f32_16x16x32_bf16 v[72:75], v[28:31], v[100:103], 0\n"
    "ds_read_b128 v[68:71], v0 offset:5120\n"
    "v_swap_b32 v59, v62\n"
    "s_waitcnt lgkmcnt(3)\n"
    "v_mfma_f32_16x16x32_bf16 v[64:67], v[96:99], v[20:23], 0\n"
    "ds_read_b128 v[28:31], v0 offset:13376\n"
    "v_swap_b32 v61, v62\n"
    "v_mfma_f32_16x16x32_bf16 v[24:27], v[96:99], v[100:103], 0\n"
    "ds_read_b128 v[20:23], v117 offset:640\n"
    "s_waitcnt lgkmcnt(3)\n"
    "v_mfma_f32_16x16x32_bf16 v[92:95], v[80:83], v[88:91], v[92:95]\n"
    "ds_read_b128 v[16:19], v0 offset:2048\n"
    "v_swap_b32 v49, v50\n"
    "s_waitcnt lgkmcnt(3)\n"
    "v_mfma_f32_16x16x32_bf16 v[84:87], v[80:83], v[68:71], v[84:87]\n"
    "ds_read_b128 v[100:103], v0 offset:10240\n"
    "v_swap_b32 v50, v52\n"
    "s_waitcnt lgkmcnt(3)\n"
    "v_mfma_f32_16x16x32_bf16 v[72:75], v[28:31], v[68:71], v[72:75]\n"
    "ds_read_b128 v[96:99], v0 offset:6144\n"
    "rgo0_%=:\n"
    "s_waitcnt vmcnt(2)\n"
    "v_mfma_f32_16x16x32_bf16 v[108:111], v[120:123], v[108:111], 0\n"
    "s_waitcnt vmcnt(1)\n"
    "v_mfma_f32_16x16x32_bf16 v[108:111], v[120:123], v[104:107], v[108:111]\n"
    "s_nop 3\n"
    "v_mov_b32 v0, v0\n"
    "s_nop 2\n"
    "v_add_f32_dpp v108, v108, v108 row_ror:8 row_mask:0xf bank_mask:0xf\n"
    "v_add_f32_dpp v109, v109, v109 row_ror:8 row_mask:0xf bank_mask:0xf\n"
    "v_cmp_u_f32 vcc, v108, v109\n"
    "v_add_f32_dpp v108, v108, v108 row_ror:4 row_mask:0xf bank_mask:0xf\n"
    "v_add_f32_dpp v109, v109, v109 row_ror:4 row_mask:0xf bank_mask:0xf\n"
    "v_cvt_pk_bf16_f32 v110, v108, v109\n"
    "s_cbranch_vccnz slowR0_%=\n"
    "rok0_%=:\n"
    "buffer_store_dword v110, v78, s[8:11], s47 offen sc1\n"
    "v_swap_b32 v51, v54\n"
    "s_waitcnt lgkmcnt(3)\n"
    "v_mfma_f32_16x16x32_bf16 v[64:67], v[20:23], v[88:91], v[64:67]\n"
    "ds_read_b128 v[80:83], v0 offset:14400\n"
    "v_swap_b32 v53, v54\n"
    "v_mfma_f32_16x16x32_bf16 v[24:27], v[20:23], v[68:71], v[24:27]\n"
    "ds_read_b128 v[88:91], v117 offset:704\n"
    "s_waitcnt lgkmcnt(3)\n"
    "v_mfma_f32_16x16x32_bf16 v[92:95], v[100:103], v[16:19], v[92:95]\n"
    "ds_read_b128 v[28:31], v0 offset:3072\n"
    "v_swap_b32 v41, v42\n"
    "s_waitcnt lgkmcnt(3)\n"
    "v_mfma_f32_16x16x32_bf16 v[84:87], v[100:103], v[96:99], v[84:87]\n"
    "ds_read_b128 v[68:71], v0 offset:11264\n"
    "v_swap_b32 v42, v44\n"
    "s_waitcnt lgkmcnt(3)\n"
    "v_mfma_f32_16x16x32_bf16 v[72:75], v[80:83], v[96:99], v[72:75]\n"
    "ds_read_b128 v[100:103], v0 offset:7168\n"
    "v_swap_b32 v43, v46\n"
    "s_waitcnt lgkmcnt(3)\n"
    "v_mfma_f32_16x16x32_bf16 v[64:67], v[88:91], v[16:19], v[64:67]\n"
    "ds_read_b128 v[80:83], v0 offset:15424\n"
    "v_swap_b32 v45, v46\n"
    "v_mfma_f32_16x16x32_bf16 v[24:27], v[88:91], v[96:99], v[24:27]\n"
    "ds_read_b128 v[20:23], v117 offset:768\n"
    "s_waitcnt lgkmcnt(3)\n"
    "v_mfma_f32_16x16x32_bf16 v[92:95], v[68:71], v[28:31], v[92:95]\n"
    "s_waitcnt lgkmcnt(2)\n"
    "v_swap_b32 v33, v34\n"
    "v_mfma_f32_16x16x32_bf16 v[84:87], v[68:71], v[100:103], v[84:87]\n"
    "s_waitcnt lgkmcnt(1)\n"
    "v_swap_b32 v34, v36\n"
    "v_mfma_f32_16x16x32_bf16 v[72:75], v[80:83], v[100:103], v[72:75]\n"
    "s_waitcnt lgkmcnt(0)\n"
    "v_swap_b32 v35, v38\n"
    "v_mfma_f32_16x16x32_bf16 v[64:67], v[20:23], v[28:31], v[64:67]\n"
    "s_waitcnt lgkmcnt(0)\n"
    "s_barrier\n"
    "s_add_u32 s45, s45, 0x10000\n"
    "v_swap_b32 v37, v38\n"
    "s_lshl_b32 m0, s67, 1\n"
    "s_add_u32 s62, s45, 0\n"
    "v_mfma_f32_16x16x32_bf16 v[24:27], v[20:23], v[100:103], v[24:27]\n"
    "buffer_load_dwordx4 v0, s[8:11], s62 offen sc1 lds\n"
    "buffer_load_dwordx4 v0, s[8:11], s62 offen offset:1024 sc1 lds\n"
    "v_mov_b32 v2, 0xff800000\n"
    "v_cndmask_b32 v92, v2, v92, s[72:73]\n"
    "v_cndmask_b32 v93, v2, v93, s[74:75]\n"
    "v_cndmask_b32 v94, v2, v94, s[76:77]\n"
    "v_cndmask_b32 v95, v2, v95, s[78:79]\n"
    "v_cndmask_b32 v72, v2, v72, s[72:73]\n"
    "v_cndmask_b32 v73, v2, v73, s[74:75]\n"
    "v_cndmask_b32 v74, v2, v74, s[76:77]\n"
    "v_cndmask_b32 v75, v2, v75, s[78:79]\n"
    "v_max_f32 v4, v92, v93\n"
    "v_max3_f32 v4, v4, v94, v95\n"
    "v_max3_f32 v4, v4, v64, v65\n"
    "v_max_f32 v5, v84, v85\n"
    "v_max3_f32 v5, v5, v86, v87\n"
    "v_max3_f32 v5, v5, v72, v73\n"
    "v_max3_f32 v5, v5, v74, v75\n"
    "v_max3_f32 v5, v5, v24, v25\n"
    "v_and_b32 v2, 0xf0, v0\n"
    "v_lshrrev_b32 v8, 4, v0\n"
    "v_permlane32_swap_b32 v4, v5\n"
    "v_max_f32 v4, v4, v5\n"
    "v_mov_b32 v5, v4\n"
    "v_and_b32 v8, 48, v8\n"
    "v_xor_b32 v8, v8, v2\n"
    "v_permlane16_swap_b32 v4, v5\n"
    "v_max_f32 v4, v4, v5\n"
    "v_mul_f32 v5, s32, v4\n"
    "v_mul_f32 v4, s32, v4\n"
    "v_lshl_or_b32 v2, v2, 4, v8\n"
    "v_lshrrev_b32 v6, 2, v0\n"
    "v_permlane32_swap_b32 v4, v5\n"
    "v_pk_fma_f32 v[92:93], v[92:93], s[32:33], v[4:5] op_sel:[0,0,0] op_sel_hi:[1,0,0] neg_lo:[0,0,1] neg_hi:[0,0,1]\n"
    "v_pk_fma_f32 v[94:95], v[94:95], s[32:33], v[4:5] op_sel:[0,0,0] op_sel_hi:[1,0,0] neg_lo:[0,0,1] neg_hi:[0,0,1]\n"
    "v_pk_fma_f32 v[64:65], v[64:65], s[32:33], v[4:5] op_sel:[0,0,0] op_sel_hi:[1,0,0] neg_lo:[0,0,1] neg_hi:[0,0,1]\n"
    "v_pk_fma_f32 v[84:85], v[84:85], s[32:33], v[4:5] op_sel:[0,0,1] op_sel_hi:[1,0,1] neg_lo:[0,0,1] neg_hi:[0,0,1]\n"
    "v_pk_fma_f32 v[86:87], v[86:87], s[32:33], v[4:5] op_sel:[0,0,1] op_sel_hi:[1,0,1] neg_lo:[0,0,1] neg_hi:[0,0,1]\n"
    "v_pk_fma_f32 v[72:73], v[72:73], s[32:33], v[4:5] op_sel:[0,0,1] op_sel_hi:[1,0,1] neg_lo:[0,0,1] neg_hi:[0,0,1]\n"
    "v_pk_fma_f32 v[74:75], v[74:75], s[32:33], v[4:5] op_sel:[0,0,1] op_sel_hi:[1,0,1] neg_lo:[0,0,1] neg_hi:[0,0,1]\n"
    "v_pk_fma_f32 v[24:25], v[24:25], s[32:33], v[4:5] op_sel:[0,0,1] op_sel_hi:[1,0,1] neg_lo:[0,0,1] neg_hi:[0,0,1]\n"
    "v_exp_f32 v92, v92\n"
    "v_exp_f32 v93, v93\n"
    "v_exp_f32 v94, v94\n"
    "v_exp_f32 v95, v95\n"
    "v_exp_f32 v64, v64\n"
    "v_exp_f32 v65, v65\n"
    "v_exp_f32 v84, v84\n"
    "v_exp_f32 v85, v85\n"
    "v_exp_f32 v86, v86\n"
    "v_exp_f32 v87, v87\n"
    "v_exp_f32 v72, v72\n"
    "v_exp_f32 v73, v73\n"
    "v_exp_f32 v74, v74\n"
    "v_exp_f32 v75, v75\n"
    "v_exp_f32 v24, v24\n"
    "v_exp_f32 v25, v25\n"
    "v_pk_add_f32 v[66:67], v[92:93], v[94:95]\n"
    "v_pk_add_f32 v[66:67], v[66:67], v[64:65]\n"
    "v_add_f32 v4, v66, v67\n"
    "v_pk_add_f32 v[26:27], v[84:85], v[86:87]\n"
    "v_pk_add_f32 v[26:27], v[26:27], v[72:73]\n"
    "v_pk_add_f32 v[26:27], v[26:27], v[74:75]\n"
    "v_pk_add_f32 v[26:27], v[26:27], v[24:25]\n"
    "v_add_f32 v5, v26, v27\n"
    "v_mov_b64 v[68:69], -1\n"
    "v_mov_b64 v[70:71], -1\n"
    "v_permlane32_swap_b32 v4, v5\n"
    "v_add_f32 v4, v4, v5\n"
    "v_mov_b32 v5, v4\n"
    "v_mov_b32 v117, 0\n"
    "v_mov_b32 v118, 0\n"
    "v_permlane16_swap_b32 v4, v5\n"
    "v_add_f32 v4, v4, v5\n"
    "v_rcp_f32 v4, v4\n"
    "s_nop 0\n"
    "v_mov_b32 v5, v4\n"
    "v_mov_b32 v103, 0\n"
    "s_nop 0\n"
    "v_permlane32_swap_b32 v4, v5\n"
    "v_pk_mul_f32 v[92:93], v[92:93], v[4:5] op_sel:[0,0] op_sel_hi:[1,0]\n"
    "v_pk_mul_f32 v[94:95], v[94:95], v[4:5] op_sel:[0,0] op_sel_hi:[1,0]\n"
    "v_pk_mul_f32 v[64:65], v[64:65], v[4:5] op_sel:[0,0] op_sel_hi:[1,0]\n"
    "v_pk_mul_f32 v[84:85], v[84:85], v[4:5] op_sel:[0,1] op_sel_hi:[1,1]\n"
    "v_pk_mul_f32 v[86:87], v[86:87], v[4:5] op_sel:[0,1] op_sel_hi:[1,1]\n"
    "v_pk_mul_f32 v[72:73], v[72:73], v[4:5] op_sel:[0,1] op_sel_hi:[1,1]\n"
    "v_pk_mul_f32 v[74:75], v[74:75], v[4:5] op_sel:[0,1] op_sel_hi:[1,1]\n"
    "v_pk_mul_f32 v[24:25], v[24:25], v[4:5] op_sel:[0,1] op_sel_hi:[1,1]\n"
    "s_mov_b32 s14, 0x3400000\n"
    "v_cvt_pk_bf16_f32 v28, v84, v85\n"
    "v_cvt_pk_bf16_f32 v29, v86, v87\n"
    "v_cvt_pk_bf16_f32 v30, v72, v73\n"
    "v_cvt_pk_bf16_f32 v31, v74, v75\n"
    "s_add_u32 m0, m0, 0x2000\n"
    "s_add_u32 s62, s45, 0x4000\n"
    "v_mfma_f32_16x16x32_bf16 v[120:123], v[28:31], v[56:59], 0\n"
    "buffer_load_dwordx4 v0, s[8:11], s62 offen sc1 lds\n"
    "v_mfma_f32_16x16x32_bf16 v[108:111], v[28:31], v[60:63], 0\n"
    "v_cvt_pk_bf16_f32 v116, v24, v25\n"
    "v_cvt_pk_bf16_f32 v119, v24, v25\n"
    "v_mfma_f32_16x16x32_bf16 v[104:107], v[28:31], v[48:51], 0\n"
    "buffer_store_dwordx4 v[68:71], v0, s[8:11], s50 offen sc1\n"
    "v_mfma_f32_16x16x32_bf16 v[96:99], v[28:31], v[52:55], 0\n"
    "v_cvt_pk_bf16_f32 v100, v92, v93\n"
    "v_cvt_pk_bf16_f32 v101, v94, v95\n"
    "v_mfma_f32_16x16x32_bf16 v[88:91], v[28:31], v[40:43], 0\n"
    "buffer_store_dwordx4 v[68:71], v0, s[8:11], s50 offen offset:1024 sc1\n"
    "s_add_u32 s61, s46, s49\n"
    "v_mfma_f32_16x16x32_bf16 v[92:95], v[28:31], v[44:47], 0\n"
    "v_cvt_pk_bf16_f32 v102, v64, v65\n"
    "v_mov_b32 v58, v124\n"
    "v_mfma_f32_16x16x32_bf16 v[84:87], v[28:31], v[32:35], 0\n"
    "buffer_store_dword v68, v6, s[8:11], s61 offen sc1\n"
    "v_mfma_f32_16x16x32_bf16 v[80:83], v[28:31], v[36:39], 0\n"
    "buffer_load_dwordx4 v0, s[8:11], s62 offen offset:1024 sc1 lds\n"
    "v_mfma_f32_16x16x16_bf16 v[120:123], v[116:117], v[124:125], v[120:123]\n"
    "v_mov_b32 v62, v125\n"
    "v_mov_b32 v50, v126\n"
    "v_mov_b32 v54, v127\n"
    "v_mfma_f32_16x16x16_bf16 v[108:111], v[118:119], v[124:125], v[108:111]\n"
    "v_mov_b32 v42, v112\n"
    "v_mov_b32 v46, v113\n"
    "v_mov_b32 v34, v114\n"
    "v_mfma_f32_16x16x16_bf16 v[104:107], v[116:117], v[126:127], v[104:107]\n"
    "v_mov_b32 v38, v115\n"
    "v_mfma_f32_16x16x16_bf16 v[96:99], v[118:119], v[126:127], v[96:99]\n"
    "v_cvt_pk_bf16_f32 v72, v120, v108\n"
    "v_cvt_pk_bf16_f32 v68, v121, v109\n"
    "v_cvt_pk_bf16_f32 v64, v122, v110\n"
    "v_mfma_f32_16x16x16_bf16 v[88:91], v[116:117], v[112:113], v[88:91]\n"
    "v_cvt_pk_bf16_f32 v28, v123, v111\n"
    "v_mfma_f32_16x16x16_bf16 v[92:95], v[118:119], v[112:113], v[92:95]\n"
    "v_cvt_pk_bf16_f32 v73, v104, v96\n"
    "v_cvt_pk_bf16_f32 v69, v105, v97\n"
    "v_cvt_pk_bf16_f32 v65, v106, v98\n"
    "v_mfma_f32_16x16x16_bf16 v[84:87], v[116:117], v[114:115], v[84:87]\n"
    "v_cvt_pk_bf16_f32 v29, v107, v99\n"
    "v_mfma_f32_16x16x16_bf16 v[80:83], v[118:119], v[114:115], v[80:83]\n"
    "v_cvt_pk_bf16_f32 v74, v88, v92\n"
    "v_cvt_pk_bf16_f32 v70, v89, v93\n"
    "v_cvt_pk_bf16_f32 v66, v90, v94\n"
    "v_mfma_f32_16x16x32_bf16 v[124:127], v[100:103], v[56:59], 0\n"
    "s_waitcnt vmcnt(5)\n"
    "join_%=:\n"
    "v_cvt_pk_bf16_f32 v30, v91, v95\n"
    "s_nop 0\n"
    "v_cvt_pk_bf16_f32 v75, v84, v80\n"
    "v_mfma_f32_16x16x32_bf16 v[120:123], v[100:103], v[60:63], 0\n"
    "s_barrier\n"
    "v_cvt_pk_bf16_f32 v71, v85, v81\n"
    "v_cvt_pk_bf16_f32 v67, v86, v82\n"
    "v_mfma_f32_16x16x32_bf16 v[96:99], v[100:103], v[48:51], 0\n"
    "ds_read_b128 v[60:63], v2\n"
    "ds_read_b128 v[56:59], v2 offset:4096\n"
    "s_xor_b32 s61, s55, 0x1000\n"
    "s_add_u32 s61, s61, 0\n"
    "v_cvt_pk_bf16_f32 v31, v87, v83\n"
    "v_mfma_f32_16x16x32_bf16 v[92:95], v[100:103], v[52:55], 0\n"
    "buffer_store_dwordx4 v[72:75], v76, s[8:11], s61 offen sc1\n"
    "s_xor_b32 s61, s55, 0x1000\n"
    "s_add_u32 s61, s61, 0x8000\n"
    "s_add_u32 s69, s69, 1\n"
    "s_add_u32 s33, s69, 1\n"
    "v_mfma_f32_16x16x32_bf16 v[88:91], v[100:103], v[40:43], 0\n"
    "v_cvt_pk_bf16_f32 v72, v124, v120\n"
    "v_cvt_pk_bf16_f32 v52, v125, v121\n"
    "v_mfma_f32_16x16x32_bf16 v[84:87], v[100:103], v[44:47], 0\n"
    "buffer_store_dwordx4 v[68:71], v76, s[8:11], s61 offen sc1\n"
    "s_xor_b32 s61, s55, 0x1000\n"
    "s_add_u32 s61, s61, 0x10000\n"
    "s_xor_b32 s53, s53, 0x8000\n"
    "s_xor_b32 s54, s54, 0x8000\n"
    "v_mfma_f32_16x16x32_bf16 v[80:83], v[100:103], v[32:35], 0\n"
    "v_cvt_pk_bf16_f32 v68, v126, v122\n"
    "v_cvt_pk_bf16_f32 v48, v127, v123\n"
    "v_cvt_pk_bf16_f32 v73, v96, v92\n"
    "v_cvt_pk_bf16_f32 v53, v97, v93\n"
    "s_waitcnt lgkmcnt(1)\n"
    "v_max_u32 v9, v60, v61\n"
    "v_max3_u32 v9, v9, v62, v63\n"
    "s_waitcnt lgkmcnt(0)\n"
    "v_max3_u32 v9, v9, v56, v57\n"
    "v_max3_u32 v9, v9, v58, v59\n"
    "v_cmp_eq_u32 vcc, -1, v9\n"
    "v_mfma_f32_16x16x32_bf16 v[44:47], v[100:103], v[36:39], 0\n"
    "buffer_store_dwordx4 v[64:67], v76, s[8:11], s61 offen sc1\n"
    "s_cmp_ge_u32 s42, 60\n"
    "s_cbranch_scc1 epi0_%=\n"
    "s_cbranch_vccnz waitH_%=\n"
    "pbgo_%=:\n"
    "v_mfma_f32_16x16x32_bf16 v[120:123], a[0:3], v[60:63], 0\n"
    "v_add_u32 v5, s66, v0\n"
    "v_xor_b32 v8, 64, v2\n"
    "v_mfma_f32_16x16x32_bf16 v[116:119], a[64:67], v[60:63], 0\n"
    "v_xor_b32 v4, 0x80, v2\n"
    "v_xor_b32 v3, 0xc0, v2\n"
    "v_mfma_f32_16x16x32_bf16 v[112:115], a[0:3], v[56:59], 0\n"
    "ds_read_b128 v[64:67], v5\n"
    "ds_read_b128 v[40:43], v5 offset:16384\n"
    "v_mfma_f32_16x16x32_bf16 v[108:111], a[64:67], v[56:59], 0\n"
    "ds_read_b128 v[36:39], v8\n"
    "ds_read_b128 v[32:35], v8 offset:4096\n"
    "v_mfma_f32_16x16x32_bf16 v[124:127], a[128:131], v[60:63], 0\n"
    "v_cvt_pk_bf16_f32 v69, v98, v94\n"
    "v_cvt_pk_bf16_f32 v49, v99, v95\n"
    "v_mfma_f32_16x16x32_bf16 v[104:107], a[192:195], v[60:63], 0\n"
    "v_cvt_pk_bf16_f32 v74, v88, v84\n"
    "v_cvt_pk_bf16_f32 v54, v89, v85\n"
    "v_mfma_f32_16x16x32_bf16 v[100:103], a[128:131], v[56:59], 0\n"
    "v_cvt_pk_bf16_f32 v70, v90, v86\n"
    "v_cvt_pk_bf16_f32 v50, v91, v87\n"
    "v_mfma_f32_16x16x32_bf16 v[96:99], a[192:195], v[56:59], 0\n"
    "s_waitcnt lgkmcnt(3)\n"
    "v_cvt_pk_bf16_f32 v75, v80, v44\n"
    "v_cvt_pk_bf16_f32 v55, v81, v45\n"
    "v_mfma_f32_16x16x32_bf16 v[92:95], v[64:67], v[60:63], 0\n"
    "s_waitcnt lgkmcnt(2)\n"
    "v_cvt_pk_bf16_f32 v71, v82, v46\n"
    "v_cvt_pk_bf16_f32 v51, v83, v47\n"
    "v_mfma_f32_16x16x32_bf16 v[88:91], v[40:43], v[60:63], 0\n"
    "s_xor_b32 s61, s55, 0x1000\n"
    "s_add_u32 s61, s61, 0x18000\n"
    "v_mfma_f32_16x16x32_bf16 v[84:87], v[64:67], v[56:59], 0\n"
    "buffer_store_dwordx4 v[28:31], v76, s[12:15], s61 offen sc1\n"
    "s_add_u32 s61, s55, 0\n"
    "v_mfma_f32_16x16x32_bf16 v[80:83], v[40:43], v[56:59], 0\n"
    "buffer_store_dwordx4 v[72:75], v76, s[12:15], s61 offen sc1\n"
    "s_waitcnt lgkmcnt(1)\n"
    "v_mfma_f32_16x16x32_bf16 v[120:123], a[4:7], v[36:39], v[120:123]\n"
    "ds_read_b128 v[64:67], v5 offset:1024\n"
    "ds_read_b128 v[60:63], v5 offset:17408\n"
    "v_mfma_f32_16x16x32_bf16 v[116:119], a[68:71], v[36:39], v[116:119]\n"
    "ds_read_b128 v[72:75], v4\n"
    "ds_read_b128 v[56:59], v4 offset:4096\n"
    "s_add_u32 m0, m0, 0x2000\n"
    "s_waitcnt lgkmcnt(4)\n"
    "v_mfma_f32_16x16x32_bf16 v[112:115], a[4:7], v[32:35], v[112:115]\n"
    "s_add_u32 s62, s45, 0x8000\n"
    "v_mfma_f32_16x16x32_bf16 v[108:111], a[68:71], v[32:35], v[108:111]\n"
    "buffer_load_dwordx4 v0, s[8:11], s62 offen sc1 lds\n"
    "v_mfma_f32_16x16x32_bf16 v[124:127], a[132:135], v[36:39], v[124:127]\n"
    "v_max3_u32 v9, v9, v36, v37\n"
    "v_max3_u32 v9, v9, v38, v39\n"
    "v_mfma_f32_16x16x32_bf16 v[104:107], a[196:199], v[36:39], v[104:107]\n"
    "v_max3_u32 v9, v9, v32, v33\n"
    "v_max3_u32 v9, v9, v34, v35\n"
    "v_mfma_f32_16x16x32_bf16 v[100:103], a[132:135], v[32:35], v[100:103]\n"
    "s_add_u32 s61, s55, 0x8000\n"
    "v_mfma_f32_16x16x32_bf16 v[96:99], a[196:199], v[32:35], v[96:99]\n"
    "buffer_store_dwordx4 v[52:55], v76, s[12:15], s61 offen sc1\n"
    "s_add_u32 s61, s55, 0x10000\n"
    "s_waitcnt lgkmcnt(3)\n"
    "v_mfma_f32_16x16x32_bf16 v[92:95], v[64:67], v[36:39], v[92:95]\n"
    "buffer_store_dwordx4 v[68:71], v76, s[12:15], s61 offen sc1\n"
    "s_waitcnt lgkmcnt(2)\n"
    "v_mfma_f32_16x16x32_bf16 v[88:91], v[60:63], v[36:39], v[88:91]\n"
    "v_mfma_f32_16x16x32_bf16 v[84:87], v[64:67], v[32:35], v[84:87]\n"
    "v_mfma_f32_16x16x32_bf16 v[80:83], v[60:63], v[32:35], v[80:83]\n"
    "s_waitcnt lgkmcnt(1)\n"
    "v_mfma_f32_16x16x32_bf16 v[120:123], a[8:11], v[72:75], v[120:123]\n"
    "ds_read_b128 v[68:71], v5 offset:2048\n"
    "ds_read_b128 v[64:67], v5 offset:18432\n"
    "v_mfma_f32_16x16x32_bf16 v[116:119], a[72:75], v[72:75], v[116:119]\n"
    "s_waitcnt lgkmcnt(2)\n"
    "v_lshlrev_b32 v6, 2, v0\n"
    "v_mfma_f32_16x16x32_bf16 v[112:115], a[8:11], v[56:59], v[112:115]\n"
    "ds_read_b128 v[60:63], v3\n"
    "ds_read_b128 v[52:55], v3 offset:4096\n"
    "s_add_u32 s62, s45, 0x8000\n"
    "v_mfma_f32_16x16x32_bf16 v[108:111], a[72:75], v[56:59], v[108:111]\n"
    "buffer_load_dwordx4 v0, s[8:11], s62 offen offset:1024 sc1 lds\n"
    "v_mfma_f32_16x16x32_bf16 v[124:127], a[136:139], v[72:75], v[124:127]\n"
    "v_max3_u32 v9, v9, v72, v73\n"
    "v_max3_u32 v9, v9, v74, v75\n"
    "v_mfma_f32_16x16x32_bf16 v[104:107], a[200:203], v[72:75], v[104:107]\n"
    "v_max3_u32 v9, v9, v56, v57\n"
    "v_max3_u32 v9, v9, v58, v59\n"
    "v_mfma_f32_16x16x32_bf16 v[100:103], a[136:139], v[56:59], v[100:103]\n"
    "s_add_u32 s61, s55, 0x18000\n"
    "v_mfma_f32_16x16x32_bf16 v[96:99], a[200:203], v[56:59], v[96:99]\n"
    "buffer_store_dwordx4 v[48:51], v76, s[12:15], s61 offen sc1\n"
    "s_waitcnt lgkmcnt(3)\n"
    "v_mfma_f32_16x16x32_bf16 v[92:95], v[68:71], v[72:75], v[92:95]\n"
    "s_waitcnt lgkmcnt(2)\n"
    "v_mfma_f32_16x16x32_bf16 v[88:91], v[64:67], v[72:75], v[88:91]\n"
    "v_mfma_f32_16x16x32_bf16 v[84:87], v[68:71], v[56:59], v[84:87]\n"
    "v_mfma_f32_16x16x32_bf16 v[80:83], v[64:67], v[56:59], v[80:83]\n"
    "s_waitcnt lgkmcnt(1)\n"
    "v_mfma_f32_16x16x32_bf16 v[120:123], a[12:15], v[60:63], v[120:123]\n"
    "ds_read_b128 v[72:75], v5 offset:3072\n"
    "ds_read_b128 v[68:71], v5 offset:19456\n"
    "s_waitcnt lgkmcnt(2)\n"
    "v_mfma_f32_16x16x32_bf16 v[116:119], a[76:79], v[60:63], v[116:119]\n"
    "s_waitcnt vmcnt(10)\n"
    "s_barrier\n"
    "v_mfma_f32_16x16x32_bf16 v[112:115], a[12:15], v[52:55], v[112:115]\n"
    "ds_read_b128 v[64:67], v2 offset:8192\n"
    "ds_read_b128 v[56:59], v2 offset:12288\n"
    "v_mfma_f32_16x16x32_bf16 v[108:111], a[76:79], v[52:55], v[108:111]\n"
    "ds_read2st64_b32 v[6:7], v6 offset0:32 offset1:48\n"
    "v_max3_u32 v9, v9, v60, v61\n"
    "v_mfma_f32_16x16x32_bf16 v[124:127], a[140:143], v[60:63], v[124:127]\n"
    "v_max3_u32 v9, v9, v62, v63\n"
    "v_max3_u32 v9, v9, v52, v53\n"
    "v_mfma_f32_16x16x32_bf16 v[104:107], a[204:207], v[60:63], v[104:107]\n"
    "v_max3_u32 v9, v9, v54, v55\n"
    "v_mfma_f32_16x16x32_bf16 v[100:103], a[140:143], v[52:55], v[100:103]\n"
    "v_mfma_f32_16x16x32_bf16 v[96:99], a[204:207], v[52:55], v[96:99]\n"
    "s_waitcnt lgkmcnt(4)\n"
    "v_mfma_f32_16x16x32_bf16 v[92:95], v[72:75], v[60:63], v[92:95]\n"
    "s_waitcnt lgkmcnt(3)\n"
    "v_mfma_f32_16x16x32_bf16 v[88:91], v[68:71], v[60:63], v[88:91]\n"
    "v_mfma_f32_16x16x32_bf16 v[84:87], v[72:75], v[52:55], v[84:87]\n"
    "v_mfma_f32_16x16x32_bf16 v[80:83], v[68:71], v[52:55], v[80:83]\n"
    "s_waitcnt lgkmcnt(2)\n"
    "v_mfma_f32_16x16x32_bf16 v[120:123], a[16:19], v[64:67], v[120:123]\n"
    "s_memrealtime s[80:81]\n"
    "ds_read_b128 v[72:75], v5 offset:4096\n"
    "ds_read_b128 v[68:71], v5 offset:20480\n"
    "v_mfma_f32_16x16x32_bf16 v[116:119], a[80:83], v[64:67], v[116:119]\n"
    "ds_read_b128 v[60:63], v8 offset:8192\n"
    "ds_read_b128 v[52:55], v8 offset:12288\n"
    "s_waitcnt lgkmcnt(5)\n"
    "v_mfma_f32_16x16x32_bf16 v[112:115], a[16:19], v[56:59], v[112:115]\n"
    "v_max3_u32 v9, v9, v64, v65\n"
    "v_max3_u32 v9, v9, v66, v67\n"
    "v_mfma_f32_16x16x32_bf16 v[108:111], a[80:83], v[56:59], v[108:111]\n"
    "v_max3_u32 v9, v9, v56, v57\n"
    "v_max3_u32 v9, v9, v58, v59\n"
    "v_mfma_f32_16x16x32_bf16 v[124:127], a[144:147], v[64:67], v[124:127]\n"
    "s_waitcnt lgkmcnt(4)\n"
    "v_max3_u32 v9, v9, v6, v7\n"
    "v_cmp_eq_u32 vcc, -1, v9\n"
    "v_mfma_f32_16x16x32_bf16 v[104:107], a[208:211], v[64:67], v[104:107]\n"
    "v_mfma_f32_16x16x32_bf16 v[100:103], a[144:147], v[56:59], v[100:103]\n"
    "s_cbranch_vccnz slowE_%=\n"
    "eok_%=:\n"
    "s_lshl_b32 s61, s41, 29\n"
    "v_mfma_f32_16x16x32_bf16 v[96:99], a[208:211], v[56:59], v[96:99]\n"
    "v_or_b32 v10, s61, v77\n"
    "s_sub_u32 s61, s65, s0\n"
    "s_waitcnt lgkmcnt(3)\n"
    "v_mfma_f32_16x16x32_bf16 v[92:95], v[72:75], v[64:67], v[92:95]\n"
    "s_max_i32 s61, s61, 0\n"
    "s_min_i32 s61, s61, 34\n"
    "s_and_b32 s1, s97, 0x100\n"
    "s_waitcnt lgkmcnt(2)\n"
    "v_mfma_f32_16x16x32_bf16 v[88:91], v[68:71], v[64:67], v[88:91]\n"
    "s_or_b32 s61, s61, s1\n"
    "s_pack_ll_b32_b16 s61, s33, s61\n"
    "v_mov_b32 v6, s61\n"
    "v_mfma_f32_16x16x32_bf16 v[84:87], v[72:75], v[56:59], v[84:87]\n"
    "s_waitcnt lgkmcnt(0)\n"
    "s_mov_b32 s1, s80\n"
    "v_mov_b32 v7, s1\n"
    "v_mfma_f32_16x16x32_bf16 v[80:83], v[68:71], v[56:59], v[80:83]\n"
    "v_mfma_f32_16x16x32_bf16 v[120:123], a[20:23], v[60:63], v[120:123]\n"
    "ds_read_b128 v[76:79], v5 offset:5120\n"
    "ds_read_b128 v[72:75], v5 offset:21504\n"
    "v_mfma_f32_16x16x32_bf16 v[116:119], a[84:87], v[60:63], v[116:119]\n"
    "ds_read_b128 v[68:71], v4 offset:8192\n"
    "ds_read_b128 v[64:67], v4 offset:12288\n"
    "s_lshl_b32 m0, s67, 1\n"
    "v_mfma_f32_16x16x32_bf16 v[112:115], a[20:23], v[52:55], v[112:115]\n"
    "s_add_u32 s62, s45, 0xc000\n"
    "v_mfma_f32_16x16x32_bf16 v[108:111], a[84:87], v[52:55], v[108:111]\n"
    "buffer_load_dwordx4 v0, s[8:11], s62 offen sc1 lds\n"
    "v_mfma_f32_16x16x32_bf16 v[124:127], a[148:151], v[60:63], v[124:127]\n"
    "v_max3_u32 v9, v9, v60, v61\n"
    "v_max3_u32 v9, v9, v62, v63\n"
    "v_mfma_f32_16x16x32_bf16 v[104:107], a[212:215], v[60:63], v[104:107]\n"
    "v_max3_u32 v9, v9, v52, v53\n"
    "v_max3_u32 v9, v9, v54, v55\n"
    "v_mfma_f32_16x16x32_bf16 v[100:103], a[148:151], v[52:55], v[100:103]\n"
    "s_waitcnt vmcnt(2)\n"
    "s_waitcnt vmcnt(1)\n"
    "s_barrier\n"
    "v_mfma_f32_16x16x32_bf16 v[96:99], a[212:215], v[52:55], v[96:99]\n"
    "buffer_store_dwordx2 v[6:7], v10, s[8:11], s53 offen sc1\n"
    "s_waitcnt lgkmcnt(3)\n"
    "v_mfma_f32_16x16x32_bf16 v[92:95], v[76:79], v[60:63], v[92:95]\n"
    "s_waitcnt lgkmcnt(2)\n"
    "v_mfma_f32_16x16x32_bf16 v[88:91], v[72:75], v[60:63], v[88:91]\n"
    "v_mfma_f32_16x16x32_bf16 v[84:87], v[76:79], v[52:55], v[84:87]\n"
    "v_mfma_f32_16x16x32_bf16 v[80:83], v[72:75], v[52:55], v[80:83]\n"
    "s_waitcnt lgkmcnt(1)\n"
    "v_mfma_f32_16x16x32_bf16 v[120:123], a[24:27], v[68:71], v[120:123]\n"
    "ds_read_b128 v[60:63], v5 offset:6144\n"
    "ds_read_b128 v[76:79], v5 offset:22528\n"
    "v_mfma_f32_16x16x32_bf16 v[116:119], a[88:91], v[68:71], v[116:119]\n"
    "ds_read_b128 v[72:75], v3 offset:8192\n"
    "ds_read_b128 v[56:59], v3 offset:12288\n"
    "s_add_u32 s62, s45, 0xc000\n"
    "s_waitcnt lgkmcnt(4)\n"
    "v_mfma_f32_16x16x32_bf16 v[112:115], a[24:27], v[64:67], v[112:115]\n"
    "buffer_load_dwordx4 v0, s[8:11], s62 offen offset:1024 sc1 lds\n"
    "v_mfma_f32_16x16x32_bf16 v[108:111], a[88:91], v[64:67], v[108:111]\n"
    "v_max3_u32 v9, v9, v68, v69\n"
    "v_max3_u32 v9, v9, v70, v71\n"
    "v_mfma_f32_16x16x32_bf16 v[124:127], a[152:155], v[68:71], v[124:127]\n"
    "v_max3_u32 v9, v9, v64, v65\n"
    "v_max3_u32 v9, v9, v66, v67\n"
    "v_mfma_f32_16x16x32_bf16 v[104:107], a[216:219], v[68:71], v[104:107]\n"
    "v_mfma_f32_16x16x32_bf16 v[100:103], a[152:155], v[64:67], v[100:103]\n"
    "v_mfma_f32_16x16x32_bf16 v[96:99], a[216:219], v[64:67], v[96:99]\n"
    "s_waitcnt lgkmcnt(3)\n"
    "v_mfma_f32_16x16x32_bf16 v[92:95], v[60:63], v[68:71], v[92:95]\n"
    "s_waitcnt lgkmcnt(2)\n"
    "v_mfma_f32_16x16x32_bf16 v[88:91], v[76:79], v[68:71], v[88:91]\n"
    "v_mfma_f32_16x16x32_bf16 v[84:87], v[60:63], v[64:67], v[84:87]\n"
    "v_mfma_f32_16x16x32_bf16 v[80:83], v[76:79], v[64:67], v[80:83]\n"
    "s_waitcnt lgkmcnt(1)\n"
    "v_mfma_f32_16x16x32_bf16 v[120:123], a[28:31], v[72:75], v[120:123]\n"
    "ds_read_b128 v[68:71], v5 offset:7168\n"
    "ds_read_b128 v[60:63], v5 offset:23552\n"
    "v_mfma_f32_16x16x32_bf16 v[116:119], a[92:95], v[72:75], v[116:119]\n"
    "ds_read_b128 v[76:79], v2 offset:16384\n"
    "ds_read_b128 v[52:55], v2 offset:20480\n"
    "s_waitcnt lgkmcnt(4)\n"
    "v_mfma_f32_16x16x32_bf16 v[112:115], a[28:31], v[56:59], v[112:115]\n"
    "v_max3_u32 v9, v9, v72, v73\n"
    "v_max3_u32 v9, v9, v74, v75\n"
    "v_mfma_f32_16x16x32_bf16 v[108:111], a[92:95], v[56:59], v[108:111]\n"
    "v_max3_u32 v9, v9, v56, v57\n"
    "v_max3_u32 v9, v9, v58, v59\n"
    "v_mfma_f32_16x16x32_bf16 v[124:127], a[156:159], v[72:75], v[124:127]\n"
    "v_mfma_f32_16x16x32_bf16 v[104:107], a[220:223], v[72:75], v[104:107]\n"
    "v_mfma_f32_16x16x32_bf16 v[100:103], a[156:159], v[56:59], v[100:103]\n"
    "v_mfma_f32_16x16x32_bf16 v[96:99], a[220:223], v[56:59], v[96:99]\n"
    "s_waitcnt lgkmcnt(3)\n"
    "v_mfma_f32_16x16x32_bf16 v[92:95], v[68:71], v[72:75], v[92:95]\n"
    "s_waitcnt lgkmcnt(2)\n"
    "v_mfma_f32_16x16x32_bf16 v[88:91], v[60:63], v[72:75], v[88:91]\n"
    "v_mfma_f32_16x16x32_bf16 v[84:87], v[68:71], v[56:59], v[84:87]\n"
    "v_mfma_f32_16x16x32_bf16 v[80:83], v[60:63], v[56:59], v[80:83]\n"
    "s_waitcnt lgkmcnt(1)\n"
    "v_mfma_f32_16x16x32_bf16 v[120:123], a[32:35], v[76:79], v[120:123]\n"
    "ds_read_b128 v[48:51], v5 offset:8192\n"
    "ds_read_b128 v[44:47], v5 offset:24576\n"
    "v_mfma_f32_16x16x32_bf16 v[116:119], a[96:99], v[76:79], v[116:119]\n"
    "ds_read_b128 v[72:75], v8 offset:16384\n"
    "ds_read_b128 v[68:71], v8 offset:20480\n"
    "s_waitcnt lgkmcnt(4)\n"
    "v_mfma_f32_16x16x32_bf16 v[112:115], a[32:35], v[52:55], v[112:115]\n"
    "v_max3_u32 v9, v9, v76, v77\n"
    "v_max3_u32 v9, v9, v78, v79\n"
    "v_mfma_f32_16x16x32_bf16 v[108:111], a[96:99], v[52:55], v[108:111]\n"
    "v_max3_u32 v9, v9, v52, v53\n"
    "v_max3_u32 v9, v9, v54, v55\n"
    "v_mfma_f32_16x16x32_bf16 v[124:127], a[160:163], v[76:79], v[124:127]\n"
    "v_mfma_f32_16x16x32_bf16 v[104:107], a[224:227], v[76:79], v[104:107]\n"
    "v_mfma_f32_16x16x32_bf16 v[100:103], a[160:163], v[52:55], v[100:103]\n"
    "v_mfma_f32_16x16x32_bf16 v[96:99], a[224:227], v[52:55], v[96:99]\n"
    "s_waitcnt lgkmcnt(3)\n"
    "v_mfma_f32_16x16x32_bf16 v[92:95], v[48:51], v[76:79], v[92:95]\n"
    "s_waitcnt lgkmcnt(2)\n"
    "v_mfma_f32_16x16x32_bf16 v[88:91], v[44:47], v[76:79], v[88:91]\n"
    "v_mfma_f32_16x16x32_bf16 v[84:87], v[48:51], v[52:55], v[84:87]\n"
    "v_mfma_f32_16x16x32_bf16 v[80:83], v[44:47], v[52:55], v[80:83]\n"
    "s_waitcnt lgkmcnt(1)\n"
    "v_mfma_f32_16x16x32_bf16 v[120:123], a[36:39], v[72:75], v[120:123]\n"
    "ds_read_b128 v[64:67], v5 offset:9216\n"
    "ds_read_b128 v[56:59], v5 offset:25600\n"
    "v_mfma_f32_16x16x32_bf16 v[116:119], a[100:103], v[72:75], v[116:119]\n"
    "ds_read_b128 v[76:79], v4 offset:16384\n"
    "ds_read_b128 v[60:63], v4 offset:20480\n"
    "s_waitcnt lgkmcnt(4)\n"
    "v_mfma_f32_16x16x32_bf16 v[112:115], a[36:39], v[68:71], v[112:115]\n"
    "v_max3_u32 v9, v9, v72, v73\n"
    "v_max3_u32 v9, v9, v74, v75\n"
    "v_mfma_f32_16x16x32_bf16 v[108:111], a[100:103], v[68:71], v[108:111]\n"
    "v_max3_u32 v9, v9, v68, v69\n"
    "v_max3_u32 v9, v9, v70, v71\n"
    "v_mfma_f32_16x16x32_bf16 v[124:127], a[164:167], v[72:75], v[124:127]\n"
    "v_lshlrev_b32 v10, 3, v0\n"
    "v_and_b32 v10, 0x380, v10\n"
    "v_mfma_f32_16x16x32_bf16 v[104:107], a[228:231], v[72:75], v[104:107]\n"
    "v_mfma_f32_16x16x32_bf16 v[100:103], a[164:167], v[68:71], v[100:103]\n"
    "v_mfma_f32_16x16x32_bf16 v[96:99], a[228:231], v[68:71], v[96:99]\n"
    "s_waitcnt lgkmcnt(3)\n"
    "v_mfma_f32_16x16x32_bf16 v[92:95], v[64:67], v[72:75], v[92:95]\n"
    "s_waitcnt lgkmcnt(2)\n"
    "v_mfma_f32_16x16x32_bf16 v[88:91], v[56:59], v[72:75], v[88:91]\n"
    "v_mfma_f32_16x16x32_bf16 v[84:87], v[64:67], v[68:71], v[84:87]\n"
    "v_mfma_f32_16x16x32_bf16 v[80:83], v[56:59], v[68:71], v[80:83]\n"
    "s_waitcnt lgkmcnt(1)\n"
    "v_mfma_f32_16x16x32_bf16 v[120:123], a[40:43], v[76:79], v[120:123]\n"
    "buffer_load_dwordx2 v[6:7], v10, s[8:11], s54 offen sc1\n"
    "v_mfma_f32_16x16x32_bf16 v[116:119], a[104:107], v[76:79], v[116:119]\n"
    "ds_read_b128 v[56:59], v5 offset:10240\n"
    "ds_read_b128 v[48:51], v5 offset:26624\n"
    "s_waitcnt lgkmcnt(2)\n"
    "v_mfma_f32_16x16x32_bf16 v[112:115], a[40:43], v[60:63], v[112:115]\n"
    "ds_read_b128 v[72:75], v3 offset:16384\n"
    "ds_read_b128 v[68:71], v3 offset:20480\n"
    "v_mfma_f32_16x16x32_bf16 v[108:111], a[104:107], v[60:63], v[108:111]\n"
    "v_max3_u32 v9, v9, v76, v77\n"
    "v_max3_u32 v9, v9, v78, v79\n"
    "v_mfma_f32_16x16x32_bf16 v[124:127], a[168:171], v[76:79], v[124:127]\n"
    "v_max3_u32 v9, v9, v60, v61\n"
    "v_max3_u32 v9, v9, v62, v63\n"
    "v_mfma_f32_16x16x32_bf16 v[104:107], a[232:235], v[76:79], v[104:107]\n"
    "v_mfma_f32_16x16x32_bf16 v[100:103], a[168:171], v[60:63], v[100:103]\n"
    "v_mfma_f32_16x16x32_bf16 v[96:99], a[232:235], v[60:63], v[96:99]\n"
    "s_waitcnt lgkmcnt(3)\n"
    "v_mfma_f32_16x16x32_bf16 v[92:95], v[56:59], v[76:79], v[92:95]\n"
    "s_waitcnt lgkmcnt(2)\n"
    "v_mfma_f32_16x16x32_bf16 v[88:91], v[48:51], v[76:79], v[88:91]\n"
    "v_mfma_f32_16x16x32_bf16 v[84:87], v[56:59], v[60:63], v[84:87]\n"
    "v_mfma_f32_16x16x32_bf16 v[80:83], v[48:51], v[60:63], v[80:83]\n"
    "s_waitcnt lgkmcnt(1)\n"
    "v_mfma_f32_16x16x32_bf16 v[120:123], a[44:47], v[72:75], v[120:123]\n"
    "ds_read_b128 v[64:67], v5 offset:11264\n"
    "ds_read_b128 v[52:55], v5 offset:27648\n"
    "s_waitcnt lgkmcnt(2)\n"
    "v_mfma_f32_16x16x32_bf16 v[116:119], a[108:111], v[72:75], v[116:119]\n"
    "s_waitcnt vmcnt(1)\n"
    "s_barrier\n"
    "v_mfma_f32_16x16x32_bf16 v[112:115], a[44:47], v[68:71], v[112:115]\n"
    "ds_read_b128 v[60:63], v2\n"
    "ds_read_b128 v[56:59], v2 offset:4096\n"
    "v_mfma_f32_16x16x32_bf16 v[108:111], a[108:111], v[68:71], v[108:111]\n"
    "v_max3_u32 v9, v9, v72, v73\n"
    "v_max3_u32 v9, v9, v74, v75\n"
    "v_mfma_f32_16x16x32_bf16 v[124:127], a[172:175], v[72:75], v[124:127]\n"
    "v_max3_u32 v9, v9, v68, v69\n"
    "v_max3_u32 v9, v9, v70, v71\n"
    "v_mfma_f32_16x16x32_bf16 v[104:107], a[236:239], v[72:75], v[104:107]\n"
    "s_cmp_lg_u32 s69, 0\n"
    "s_cselect_b32 s62, s57, -1\n"
    "s_cmp_lt_u32 s62, 0x200000\n"
    "v_mfma_f32_16x16x32_bf16 v[100:103], a[172:175], v[68:71], v[100:103]\n"
    "s_cselect_b32 s6, 0x200000, 0\n"
    "s_cselect_b32 s14, 0x3400000, 0\n"
    "s_cselect_b32 s62, s57, 0\n"
    "v_mfma_f32_16x16x32_bf16 v[96:99], a[236:239], v[68:71], v[96:99]\n"
    "s_cmp_eq_u32 s40, 0\n"
    "s_cselect_b32 s14, s14, 0\n"
    "s_waitcnt lgkmcnt(3)\n"
    "v_mfma_f32_16x16x32_bf16 v[92:95], v[64:67], v[72:75], v[92:95]\n"
    "buffer_load_dwordx4 v[76:79], v0, s[4:7], s62 offen sc1\n"
    "s_waitcnt lgkmcnt(2)\n"
    "v_mfma_f32_16x16x32_bf16 v[88:91], v[52:55], v[72:75], v[88:91]\n"
    "v_lshlrev_b32 v11, 5, v0\n"
    "s_add_u32 s62, s57, s3\n"
    "v_mfma_f32_16x16x32_bf16 v[84:87], v[64:67], v[68:71], v[84:87]\n"
    "v_lshrrev_b32 v11, s99, v11\n"
    "v_mfma_f32_16x16x32_bf16 v[80:83], v[52:55], v[68:71], v[80:83]\n"
    "buffer_load_dwordx2 v[72:73], v11, s[12:15], s62 offen sc1\n"
    "s_add_u32 s62, s62, s98\n"
    "buffer_load_dwordx2 v[74:75], v11, s[12:15], s62 offen offset:256 sc1\n"
    "s_cmp_eq_u32 s69, 0\n"
    "s_cselect_b32 s61, 0x40000000, 0\n"
    "v_or_b32 v2, s61, v0\n"
    "s_waitcnt lgkmcnt(1)\n"
    "v_mfma_f32_16x16x32_bf16 v[120:123], a[48:51], v[60:63], v[120:123]\n"
    "ds_read_b128 v[52:55], v5 offset:12288\n"
    "ds_read_b128 v[48:51], v5 offset:28672\n"
    "v_mfma_f32_16x16x32_bf16 v[116:119], a[112:115], v[60:63], v[116:119]\n"
    "ds_read_b128 v[40:43], v8\n"
    "ds_read_b128 v[32:35], v8 offset:4096\n"
    "s_waitcnt lgkmcnt(4)\n"
    "v_mfma_f32_16x16x32_bf16 v[112:115], a[48:51], v[56:59], v[112:115]\n"
    "v_max3_u32 v9, v9, v60, v61\n"
    "v_max3_u32 v9, v9, v62, v63\n"
    "v_mfma_f32_16x16x32_bf16 v[108:111], a[112:115], v[56:59], v[108:111]\n"
    "v_max3_u32 v9, v9, v56, v57\n"
    "v_max3_u32 v9, v9, v58, v59\n"
    "v_mfma_f32_16x16x32_bf16 v[124:127], a[176:179], v[60:63], v[124:127]\n"
    "s_waitcnt vmcnt(3)\n"
    "v_cmp_ne_u16 vcc, s33, v6\n"
    "v_mfma_f32_16x16x32_bf16 v[104:107], a[240:243], v[60:63], v[104:107]\n"
    "v_mfma_f32_16x16x32_bf16 v[100:103], a[176:179], v[56:59], v[100:103]\n"
    "s_cbranch_vccnz slowF_%=\n"
    "flok_%=:\n"
    "s_waitcnt vmcnt(4)\n"
    "v_mfma_f32_16x16x32_bf16 v[96:99], a[240:243], v[56:59], v[96:99]\n"
    "s_barrier\n"
    "s_waitcnt lgkmcnt(3)\n"
    "v_mfma_f32_16x16x32_bf16 v[92:95], v[52:55], v[60:63], v[92:95]\n"
    "buffer_load_dwordx4 v[68:71], v2, s[8:11], s56 offen sc1\n"
    "s_waitcnt lgkmcnt(2)\n"
    "v_mfma_f32_16x16x32_bf16 v[88:91], v[48:51], v[60:63], v[88:91]\n"
    "buffer_load_dwordx4 v[64:67], v2, s[8:11], s56 offen offset:1024 sc1\n"
    "v_mfma_f32_16x16x32_bf16 v[84:87], v[52:55], v[56:59], v[84:87]\n"
    "v_mfma_f32_16x16x32_bf16 v[80:83], v[48:51], v[56:59], v[80:83]\n"
    "s_waitcnt lgkmcnt(1)\n"
    "v_mfma_f32_16x16x32_bf16 v[120:123], a[52:55], v[40:43], v[120:123]\n"
    "ds_read_b128 v[28:31], v5 offset:13312\n"
    "ds_read_b128 v[24:27], v5 offset:29696\n"
    "v_mfma_f32_16x16x32_bf16 v[116:119], a[116:119], v[40:43], v[116:119]\n"
    "s_waitcnt lgkmcnt(2)\n"
    "v_lshrrev_b32 v10, 4, v10\n"
    "v_add_u32 v10, s68, v10\n"
    "v_mfma_f32_16x16x32_bf16 v[112:115], a[52:55], v[32:35], v[112:115]\n"
    "ds_write_b64 v10, v[6:7] offset:32000\n"
    "v_mfma_f32_16x16x32_bf16 v[108:111], a[116:119], v[32:35], v[108:111]\n"
    "ds_read_b128 v[60:63], v4\n"
    "ds_read_b128 v[56:59], v4 offset:4096\n"
    "v_mfma_f32_16x16x32_bf16 v[124:127], a[180:183], v[40:43], v[124:127]\n"
    "ds_read_b128 v[52:55], v3\n"
    "ds_read_b128 v[48:51], v3 offset:4096\n"
    "v_mfma_f32_16x16x32_bf16 v[104:107], a[244:247], v[40:43], v[104:107]\n"
    "v_max3_u32 v9, v9, v40, v41\n"
    "v_max3_u32 v9, v9, v42, v43\n"
    "v_mfma_f32_16x16x32_bf16 v[100:103], a[180:183], v[32:35], v[100:103]\n"
    "v_max3_u32 v9, v9, v32, v33\n"
    "v_max3_u32 v9, v9, v34, v35\n"
    "v_mfma_f32_16x16x32_bf16 v[96:99], a[244:247], v[32:35], v[96:99]\n"
    "buffer_load_dwordx4 v[44:47], v2, s[8:11], s56 offen offset:2048 sc1\n"
    "s_waitcnt lgkmcnt(6)\n"
    "v_mfma_f32_16x16x32_bf16 v[92:95], v[28:31], v[40:43], v[92:95]\n"
    "buffer_load_dwordx4 v[36:39], v2, s[8:11], s56 offen offset:3072 sc1\n"
    "s_waitcnt lgkmcnt(5)\n"
    "v_mfma_f32_16x16x32_bf16 v[88:91], v[24:27], v[40:43], v[88:91]\n"
    "s_waitcnt lgkmcnt(3)\n"
    "v_max3_u32 v9, v9, v60, v61\n"
    "v_max3_u32 v9, v9, v62, v63\n"
    "v_mfma_f32_16x16x32_bf16 v[84:87], v[28:31], v[32:35], v[84:87]\n"
    "s_waitcnt lgkmcnt(2)\n"
    "v_max3_u32 v9, v9, v56, v57\n"
    "v_max3_u32 v9, v9, v58, v59\n"
    "v_mfma_f32_16x16x32_bf16 v[80:83], v[24:27], v[32:35], v[80:83]\n"
    "s_waitcnt lgkmcnt(1)\n"
    "v_max3_u32 v9, v9, v52, v53\n"
    "v_max3_u32 v9, v9, v54, v55\n"
    "s_waitcnt lgkmcnt(0)\n"
    "v_max3_u32 v9, v9, v48, v49\n"
    "v_max3_u32 v9, v9, v50, v51\n"
    "v_cmp_eq_u32 vcc, -1, v9\n"
    "v_mfma_f32_16x16x32_bf16 v[124:127], a[184:187], v[60:63], v[124:127]\n"
    "ds_read_b128 v[40:43], v5 offset:14336\n"
    "ds_read_b128 v[32:35], v5 offset:30720\n"
    "v_mfma_f32_16x16x32_bf16 v[104:107], a[248:251], v[60:63], v[104:107]\n"
    "s_cbranch_vccnz slowN_%=\n"
    "pbdone_%=:\n"
    "v_mfma_f32_16x16x32_bf16 v[124:127], a[188:191], v[52:55], v[124:127]\n"
    "ds_read_b128 v[28:31], v5 offset:15360\n"
    "ds_read_b128 v[24:27], v5 offset:31744\n"
    "v_mfma_f32_16x16x32_bf16 v[104:107], a[252:255], v[52:55], v[104:107]\n"
    "v_add_u32 v3, s67, v0\n"
    "v_and_b32 v4, 0xffffffdf, v0\n"
    "v_mfma_f32_16x16x32_bf16 v[100:103], a[184:187], v[56:59], v[100:103]\n"
    "buffer_load_dwordx4 v[20:23], v2, s[8:11], s35 offen sc1\n"
    "buffer_load_dwordx4 v[16:19], v2, s[8:11], s35 offen offset:1024 sc1\n"
    "v_mfma_f32_16x16x32_bf16 v[96:99], a[248:251], v[56:59], v[96:99]\n"
    "v_cvt_pk_bf16_f32 v12, v124, v104\n"
    "v_cvt_pk_bf16_f32 v13, v125, v105\n"
    "v_mfma_f32_16x16x32_bf16 v[100:103], a[188:191], v[48:51], v[100:103]\n"
    "v_cvt_pk_bf16_f32 v14, v126, v106\n"
    "v_cvt_pk_bf16_f32 v15, v127, v107\n"
    "v_mfma_f32_16x16x32_bf16 v[96:99], a[252:255], v[48:51], v[96:99]\n"
    "ds_write_b128 v3, v[12:15] offset:16448\n"
    "s_waitcnt lgkmcnt(4)\n"
    "v_mfma_f32_16x16x32_bf16 v[92:95], v[40:43], v[60:63], v[92:95]\n"
    "ds_read_b128 v[104:107], v0 offset:24640\n"
    "ds_read_b128 v[8:11], v0 offset:24640\n"
    "s_waitcnt lgkmcnt(5)\n"
    "v_mfma_f32_16x16x32_bf16 v[88:91], v[32:35], v[60:63], v[88:91]\n"
    "s_waitcnt lgkmcnt(4)\n"
    "v_cvt_pk_bf16_f32 v12, v100, v96\n"
    "v_cvt_pk_bf16_f32 v13, v101, v97\n"
    "v_mfma_f32_16x16x32_bf16 v[92:95], v[28:31], v[52:55], v[92:95]\n"
    "s_waitcnt lgkmcnt(3)\n"
    "v_cvt_pk_bf16_f32 v14, v102, v98\n"
    "v_cvt_pk_bf16_f32 v15, v103, v99\n"
    "v_mfma_f32_16x16x32_bf16 v[88:91], v[24:27], v[52:55], v[88:91]\n"
    "ds_write_b128 v3, v[12:15] offset:20544\n"
    "v_mfma_f32_16x16x32_bf16 v[84:87], v[40:43], v[56:59], v[84:87]\n"
    "buffer_load_dwordx4 v[100:103], v2, s[8:11], s35 offen offset:2048 sc1\n"
    "buffer_load_dwordx4 v[124:127], v2, s[8:11], s35 offen offset:3072 sc1\n"
    "v_mfma_f32_16x16x32_bf16 v[80:83], v[32:35], v[56:59], v[80:83]\n"
    "v_cvt_pk_bf16_f32 v96, v92, v88\n"
    "v_cvt_pk_bf16_f32 v97, v93, v89\n"
    "v_mfma_f32_16x16x32_bf16 v[84:87], v[28:31], v[48:51], v[84:87]\n"
    "v_cvt_pk_bf16_f32 v98, v94, v90\n"
    "v_cvt_pk_bf16_f32 v99, v95, v91\n"
    "v_mfma_f32_16x16x32_bf16 v[80:83], v[24:27], v[48:51], v[80:83]\n"
    "ds_write_b128 v3, v[96:99] offset:8192\n"
    "v_mfma_f32_16x16x32_bf16 v[120:123], a[56:59], v[60:63], v[120:123]\n"
    "s_waitcnt lgkmcnt(1)\n"
    "s_barrier\n"
    "v_mfma_f32_16x16x32_bf16 v[116:119], a[120:123], v[60:63], v[116:119]\n"
    "v_cvt_pk_bf16_f32 v92, v84, v80\n"
    "v_cvt_pk_bf16_f32 v93, v85, v81\n"
    "v_mfma_f32_16x16x32_bf16 v[120:123], a[60:63], v[52:55], v[120:123]\n"
    "v_cvt_pk_bf16_f32 v94, v86, v82\n"
    "v_cvt_pk_bf16_f32 v95, v87, v83\n"
    "v_mfma_f32_16x16x32_bf16 v[116:119], a[124:127], v[52:55], v[116:119]\n"
    "ds_read_b128 v[88:91], v4 offset:16448\n"
    "ds_read_b128 v[40:43], v4 offset:17472\n"
    "v_mfma_f32_16x16x32_bf16 v[112:115], a[56:59], v[56:59], v[112:115]\n"
    "ds_read_b128 v[84:87], v4 offset:18496\n"
    "ds_read_b128 v[80:83], v4 offset:19520\n"
    "v_mfma_f32_16x16x32_bf16 v[108:111], a[120:123], v[56:59], v[108:111]\n"
    "v_cvt_pk_bf16_f32 v60, v120, v116\n"
    "v_cvt_pk_bf16_f32 v61, v121, v117\n"
    "v_mfma_f32_16x16x32_bf16 v[112:115], a[60:63], v[48:51], v[112:115]\n"
    "v_cvt_pk_bf16_f32 v62, v122, v118\n"
    "v_cvt_pk_bf16_f32 v63, v123, v119\n"
    "v_mfma_f32_16x16x32_bf16 v[108:111], a[124:127], v[48:51], v[108:111]\n"
    "ds_write_b128 v3, v[92:95] offset:12352\n"
    "ds_read_b128 v[96:99], v0 offset:27712\n"
    "s_waitcnt lgkmcnt(5)\n"
    "v_smfmac_f32_16x16x64_bf16 v[104:107], v[88:91], v[128:135], v1\n"
    "ds_write_b128 v3, v[60:63]\n"
    "s_waitcnt lgkmcnt(5)\n"
    "v_smfmac_f32_16x16x64_bf16 v[104:107], v[40:43], v[136:143], v1\n"
    "s_waitcnt lgkmcnt(4)\n"
    "v_cvt_pk_bf16_f32 v92, v112, v108\n"
    "v_smfmac_f32_16x16x64_bf16 v[104:107], v[84:87], v[144:151], v1\n"
    "ds_read_b128 v[120:123], v4 offset:16480\n"
    "s_waitcnt lgkmcnt(4)\n"
    "v_cvt_pk_bf16_f32 v93, v113, v109\n"
    "v_smfmac_f32_16x16x64_bf16 v[104:107], v[80:83], v[152:159], v1\n"
    "ds_read_b128 v[32:35], v0 offset:24640\n"
    "s_waitcnt vmcnt(7) lgkmcnt(3)\n"
    "v_cvt_pk_bf16_f32 v94, v114, v110\n"
    "v_mfma_f32_16x16x32_bf16 v[116:119], v[96:99], v[68:71], 0\n"
    "v_smfmac_f32_16x16x64_bf16 v[8:11], v[88:91], v[160:167], v1\n"
    "v_cvt_pk_bf16_f32 v95, v115, v111\n"
    "v_smfmac_f32_16x16x64_bf16 v[8:11], v[40:43], v[168:175], v1\n"
    "ds_write_b128 v3, v[92:95] offset:4096\n"
    "v_smfmac_f32_16x16x64_bf16 v[8:11], v[84:87], v[176:183], v1\n"
    "ds_read_b128 v[112:115], v4 offset:17504\n"
    "v_cvt_pk_bf16_f32 v56, v104, v105\n"
    "v_smfmac_f32_16x16x64_bf16 v[8:11], v[80:83], v[184:191], v1\n"
    "ds_read_b128 v[108:111], v0 offset:24640\n"
    "s_waitcnt vmcnt(6)\n"
    "v_cvt_pk_bf16_f32 v57, v106, v107\n"
    "v_mfma_f32_16x16x32_bf16 v[116:119], v[96:99], v[64:67], v[116:119]\n"
    "s_waitcnt lgkmcnt(3)\n"
    "v_lshrrev_b32 v2, 1, v0\n"
    "v_and_b32 v2, 0xf8, v2\n"
    "v_smfmac_f32_16x16x64_bf16 v[32:35], v[88:91], v[192:199], v1\n"
    "ds_read_b64 v[2:3], v2 offset:32000\n"
    "v_bfe_u32 v5, v0, 8, 1\n"
    "v_smfmac_f32_16x16x64_bf16 v[32:35], v[40:43], v[200:207], v1\n"
    "s_cmp_eq_u32 s40, 0\n"
    "s_cselect_b32 s63, 0x41, 0\n"
    "s_cselect_b32 s61, 0x4b, 55\n"
    "v_lshlrev_b32 v5, 9, v5\n"
    "v_smfmac_f32_16x16x64_bf16 v[32:35], v[84:87], v[208:215], v1\n"
    "ds_read_b128 v[104:107], v4 offset:18528\n"
    "v_cvt_pk_bf16_f32 v48, v8, v9\n"
    "v_smfmac_f32_16x16x64_bf16 v[32:35], v[80:83], v[216:223], v1\n"
    "ds_read_b128 v[92:95], v0 offset:24640\n"
    "s_waitcnt vmcnt(5)\n"
    "v_cvt_pk_bf16_f32 v49, v10, v11\n"
    "v_mfma_f32_16x16x32_bf16 v[116:119], v[96:99], v[44:47], v[116:119]\n"
    "s_cmp_eq_u32 s69, 0\n"
    "s_cselect_b32 s63, s61, s63\n"
    "v_bfe_u32 v6, v0, 2, 6\n"
    "s_cmp_eq_u32 s42, 2\n"
    "s_cselect_b32 s61, 20, 0\n"
    "s_add_u32 s63, s63, s61\n"
    "v_add3_u32 v5, v5, s68, v6\n"
    "s_waitcnt lgkmcnt(3)\n"
    "v_smfmac_f32_16x16x64_bf16 v[108:111], v[88:91], v[224:231], v1\n"
    "s_cmp_eq_u32 s41, 0\n"
    "s_cselect_b32 s61, 0x18c0, 0\n"
    "v_add_u32 v6, s61, v0\n"
    "v_smfmac_f32_16x16x64_bf16 v[108:111], v[40:43], v[232:239], v1\n"
    "s_add_u32 s63, s63, 0xe1\n"
    "s_waitcnt lgkmcnt(2)\n"
    "v_subrev_u32 v3, s1, v3\n"
    "v_smfmac_f32_16x16x64_bf16 v[108:111], v[84:87], v[240:247], v1\n"
    "ds_read_b128 v[88:91], v4 offset:19552\n"
    "v_cvt_pk_bf16_f32 v40, v32, v33\n"
    "v_smfmac_f32_16x16x64_bf16 v[108:111], v[80:83], v[248:255], v1\n"
    "ds_read_b128 v[84:87], v0 offset:24640\n"
    "s_waitcnt vmcnt(4)\n"
    "v_cvt_pk_bf16_f32 v41, v34, v35\n"
    "v_mfma_f32_16x16x32_bf16 v[116:119], v[96:99], v[36:39], v[116:119]\n"
    "s_add_u32 s1, s1, s63\n"
    "v_max_i32_dpp v2, v2, v2 row_ror:8 row_mask:0xf bank_mask:0xf\n"
    "v_max_i32_dpp v3, v3, v3 row_ror:8 row_mask:0xf bank_mask:0xf\n"
    "s_waitcnt lgkmcnt(2)\n"
    "v_smfmac_f32_16x16x64_bf16 v[92:95], v[120:123], v[128:135], v1\n"
    "v_max_i32_dpp v2, v2, v2 row_ror:4 row_mask:0xf bank_mask:0xf\n"
    "v_smfmac_f32_16x16x64_bf16 v[92:95], v[112:115], v[136:143], v1\n"
    "v_max_i32_dpp v3, v3, v3 row_ror:4 row_mask:0xf bank_mask:0xf\n"
    "v_smfmac_f32_16x16x64_bf16 v[92:95], v[104:107], v[144:151], v1\n"
    "ds_read_b128 v[80:83], v4 offset:20544\n"
    "s_waitcnt lgkmcnt(2)\n"
    "v_cvt_pk_bf16_f32 v32, v108, v109\n"
    "v_smfmac_f32_16x16x64_bf16 v[92:95], v[88:91], v[152:159], v1\n"
    "ds_read_b128 v[68:71], v0 offset:24640\n"
    "s_waitcnt vmcnt(3)\n"
    "v_cvt_pk_bf16_f32 v33, v110, v111\n"
    "v_mfma_f32_16x16x32_bf16 v[116:119], v[96:99], v[20:23], v[116:119]\n"
    "s_waitcnt lgkmcnt(2)\n"
    "v_max_i32_dpp v2, v2, v2 row_ror:2 row_mask:0xf bank_mask:0xf\n"
    "v_max_i32_dpp v3, v3, v3 row_ror:2 row_mask:0xf bank_mask:0xf\n"
    "v_smfmac_f32_16x16x64_bf16 v[84:87], v[120:123], v[160:167], v1\n"
    "v_max_i32_dpp v2, v2, v2 row_ror:1 row_mask:0xf bank_mask:0xf\n"
    "v_smfmac_f32_16x16x64_bf16 v[84:87], v[112:115], v[168:175], v1\n"
    "v_max_i32_dpp v3, v3, v3 row_ror:1 row_mask:0xf bank_mask:0xf\n"
    "v_smfmac_f32_16x16x64_bf16 v[84:87], v[104:107], v[176:183], v1\n"
    "ds_read_b128 v[108:111], v4 offset:21568\n"
    "v_cvt_pk_bf16_f32 v58, v92, v93\n"
    "v_smfmac_f32_16x16x64_bf16 v[84:87], v[88:91], v[184:191], v1\n"
    "ds_read_b128 v[64:67], v0 offset:24640\n"
    "s_waitcnt vmcnt(2)\n"
    "v_cvt_pk_bf16_f32 v59, v94, v95\n"
    "v_mfma_f32_16x16x32_bf16 v[116:119], v[96:99], v[16:19], v[116:119]\n"
    "s_waitcnt lgkmcnt(2)\n"
    "v_readlane_b32 s61, v2, 0\n"
    "v_readlane_b32 s62, v2, 16\n"
    "v_smfmac_f32_16x16x64_bf16 v[68:71], v[120:123], v[192:199], v1\n"
    "v_readlane_b32 s63, v3, 0\n"
    "v_smfmac_f32_16x16x64_bf16 v[68:71], v[112:115], v[200:207], v1\n"
    "s_max_i32 s61, s61, s62\n"
    "s_lshr_b32 s61, s61, 16\n"
    "v_mov_b32 v0, v0\n"
    "v_smfmac_f32_16x16x64_bf16 v[68:71], v[104:107], v[208:215], v1\n"
    "ds_read_b128 v[92:95], v4 offset:22592\n"
    "v_cvt_pk_bf16_f32 v50, v84, v85\n"
    "v_smfmac_f32_16x16x64_bf16 v[68:71], v[88:91], v[216:223], v1\n"
    "ds_read_b128 v[28:31], v0 offset:24640\n"
    "s_waitcnt vmcnt(1)\n"
    "v_cvt_pk_bf16_f32 v51, v86, v87\n"
    "v_mfma_f32_16x16x32_bf16 v[116:119], v[96:99], v[100:103], v[116:119]\n"
    "s_lshr_b32 s65, s61, 8\n"
    "s_and_b32 s61, s61, 0xff\n"
    "s_and_b32 s97, s97, 0xff\n"
    "v_mov_b32 v0, v0\n"
    "s_cmp_gt_u32 s69, 2\n"
    "s_cselect_b32 s65, s65, 0\n"
    "s_add_u32 s97, s97, s65\n"
    "s_min_u32 s97, s97, 1\n"
    "v_readlane_b32 s62, v3, 16\n"
    "s_waitcnt lgkmcnt(2)\n"
    "v_smfmac_f32_16x16x64_bf16 v[64:67], v[120:123], v[224:231], v1\n"
    "s_cmp_eq_u32 s42, 0\n"
    "s_cselect_b32 s65, 4, 0\n"
    "s_mul_i32 s65, s40, s65\n"
    "s_add_u32 s65, s65, 0x78\n"
    "v_mov_b32 v0, v0\n"
    "v_smfmac_f32_16x16x64_bf16 v[64:67], v[112:115], v[232:239], v1\n"
    "s_cmp_eq_u32 s69, 1\n"
    "s_cselect_b32 s0, 0, 30\n"
    "s_cselect_b32 s64, 33, 32\n"
    "v_mov_b32 v0, v0\n"
    "v_smfmac_f32_16x16x64_bf16 v[64:67], v[104:107], v[240:247], v1\n"
    "ds_read_b128 v[120:123], v4 offset:23616\n"
    "v_cvt_pk_bf16_f32 v42, v68, v69\n"
    "v_smfmac_f32_16x16x64_bf16 v[64:67], v[88:91], v[248:255], v1\n"
    "ds_read_b128 v[112:115], v0 offset:24640\n"
    "s_waitcnt vmcnt(0)\n"
    "v_cvt_pk_bf16_f32 v43, v70, v71\n"
    "v_mfma_f32_16x16x32_bf16 v[116:119], v[96:99], v[124:127], v[116:119]\n"
    "s_cselect_b32 s37, s65, s37\n"
    "s_add_u32 s71, s61, 33\n"
    "s_lshr_b32 s71, s71, 1\n"
    "s_min_i32 s0, s0, s71\n"
    "v_mov_b32 v0, v0\n"
    "s_max_i32 s61, s61, s0\n"
    "s_add_u32 s37, s37, s61\n"
    "s_sub_u32 s37, s37, s64\n"
    "v_mov_b32 v0, v0\n"
    "s_waitcnt lgkmcnt(2)\n"
    "v_smfmac_f32_16x16x64_bf16 v[28:31], v[80:83], v[128:135], v1\n"
    "ds_read_b128 v[104:107], v4 offset:20576\n"
    "v_cvt_pk_bf16_f32 v34, v64, v65\n"
    "v_smfmac_f32_16x16x64_bf16 v[28:31], v[108:111], v[136:143], v1\n"
    "ds_read_b128 v[124:127], v0 offset:24640\n"
    "v_cvt_pk_bf16_f32 v116, v116, v117\n"
    "v_cvt_pk_bf16_f32 v117, v118, v119\n"
    "v_smfmac_f32_16x16x64_bf16 v[28:31], v[92:95], v[144:151], v1\n"
    "ds_write2st64_b32 v5, v116, v117 offset0:121 offset1:122\n"
    "s_waitcnt lgkmcnt(4)\n"
    "v_smfmac_f32_16x16x64_bf16 v[28:31], v[120:123], v[152:159], v1\n"
    "s_waitcnt lgkmcnt(3)\n"
    "v_cvt_pk_bf16_f32 v35, v66, v67\n"
    "v_smfmac_f32_16x16x64_bf16 v[112:115], v[80:83], v[160:167], v1\n"
    "s_cmp_eq_u32 s69, 1\n"
    "s_cselect_b32 s71, 2, -16\n"
    "s_cselect_b32 s64, 60, 60\n"
    "v_mov_b32 v0, v0\n"
    "v_smfmac_f32_16x16x64_bf16 v[112:115], v[108:111], v[168:175], v1\n"
    "ds_read_b32 v8, v0 offset:29772\n"
    "s_waitcnt lgkmcnt(1)\n"
    "s_barrier\n"
    "ds_read_b128 v[100:103], v6 offset:24640\n"
    "v_smfmac_f32_16x16x64_bf16 v[112:115], v[92:95], v[176:183], v1\n"
    "ds_read_b128 v[116:119], v4 offset:21600\n"
    "v_cvt_pk_bf16_f32 v60, v28, v29\n"
    "v_smfmac_f32_16x16x64_bf16 v[112:115], v[120:123], v[184:191], v1\n"
    "ds_read_b128 v[96:99], v0 offset:24640\n"
    "v_cvt_pk_bf16_f32 v61, v30, v31\n"
    "v_smfmac_f32_16x16x64_bf16 v[124:127], v[80:83], v[192:199], v1\n"
    "s_sub_u32 s37, s37, 0x6e\n"
    "s_max_i32 s37, s37, s71\n"
    "s_min_i32 s0, s37, s64\n"
    "v_mov_b32 v0, v0\n"
    "v_smfmac_f32_16x16x64_bf16 v[124:127], v[108:111], v[200:207], v1\n"
    "s_add_u32 s37, s0, 0x6e\n"
    "s_ashr_i32 s0, s0, 1\n"
    "v_mov_b32 v0, v0\n"
    "v_smfmac_f32_16x16x64_bf16 v[124:127], v[92:95], v[208:215], v1\n"
    "ds_read_b128 v[88:91], v4 offset:22624\n"
    "v_cvt_pk_bf16_f32 v52, v112, v113\n"
    "v_smfmac_f32_16x16x64_bf16 v[124:127], v[120:123], v[216:223], v1\n"
    "ds_read_b128 v[84:87], v0 offset:24640\n"
    "s_waitcnt lgkmcnt(2)\n"
    "v_cvt_pk_bf16_f32 v53, v114, v115\n"
    "v_smfmac_f32_16x16x64_bf16 v[96:99], v[80:83], v[224:231], v1\n"
    "s_cmp_eq_u32 s69, 0\n"
    "s_cselect_b32 s61, 14, s0\n"
    "s_cselect_b32 s0, 0, s0\n"
    "v_mov_b32 v0, v0\n"
    "v_smfmac_f32_16x16x64_bf16 v[96:99], v[108:111], v[232:239], v1\n"
    "s_sub_u32 s65, s97, s0\n"
    "s_add_u32 s65, s65, 35\n"
    "v_mov_b32 v0, v0\n"
    "v_smfmac_f32_16x16x64_bf16 v[96:99], v[92:95], v[240:247], v1\n"
    "ds_read_b128 v[112:115], v4 offset:23648\n"
    "v_cvt_pk_bf16_f32 v44, v124, v125\n"
    "v_smfmac_f32_16x16x64_bf16 v[96:99], v[120:123], v[248:255], v1\n"
    "ds_read_b128 v[108:111], v0 offset:24640\n"
    "s_waitcnt lgkmcnt(2)\n"
    "v_cvt_pk_bf16_f32 v45, v126, v127\n"
    "v_smfmac_f32_16x16x64_bf16 v[84:87], v[104:107], v[128:135], v1\n"
    "s_cmp_eq_u32 s69, 1\n"
    "s_cselect_b32 s71, 62, 58\n"
    "s_add_u32 s61, s61, s71\n"
    "v_mov_b32 v0, v0\n"
    "v_smfmac_f32_16x16x64_bf16 v[84:87], v[116:119], v[136:143], v1\n"
    "s_add_u32 s71, s37, s61\n"
    "s_max_i32 s62, s62, s63\n"
    "s_add_u32 s1, s1, s62\n"
    "v_lshlrev_b32 v120, 16, v76\n"
    "v_smfmac_f32_16x16x64_bf16 v[84:87], v[88:91], v[144:151], v1\n"
    "ds_read_b128 v[92:95], v0 offset:24640\n"
    "s_waitcnt lgkmcnt(2)\n"
    "v_cvt_pk_bf16_f32 v36, v96, v97\n"
    "v_smfmac_f32_16x16x64_bf16 v[84:87], v[112:115], v[152:159], v1\n"
    "s_waitcnt lgkmcnt(1)\n"
    "v_cvt_pk_bf16_f32 v37, v98, v99\n"
    "v_smfmac_f32_16x16x64_bf16 v[108:111], v[104:107], v[160:167], v1\n"
    "v_and_b32 v121, 0xffff0000, v76\n"
    "v_smfmac_f32_16x16x64_bf16 v[108:111], v[116:119], v[168:175], v1\n"
    "v_lshlrev_b32 v122, 16, v77\n"
    "v_smfmac_f32_16x16x64_bf16 v[108:111], v[88:91], v[176:183], v1\n"
    "ds_read_b128 v[96:99], v0 offset:24640\n"
    "v_cvt_pk_bf16_f32 v62, v84, v85\n"
    "v_smfmac_f32_16x16x64_bf16 v[108:111], v[112:115], v[184:191], v1\n"
    "s_waitcnt lgkmcnt(1)\n"
    "v_cvt_pk_bf16_f32 v63, v86, v87\n"
    "v_smfmac_f32_16x16x64_bf16 v[92:95], v[104:107], v[192:199], v1\n"
    "v_and_b32 v123, 0xffff0000, v77\n"
    "v_smfmac_f32_16x16x64_bf16 v[92:95], v[116:119], v[200:207], v1\n"
    "v_lshlrev_b32 v124, 16, v78\n"
    "v_smfmac_f32_16x16x64_bf16 v[92:95], v[88:91], v[208:215], v1\n"
    "v_cvt_pk_bf16_f32 v54, v108, v109\n"
    "v_smfmac_f32_16x16x64_bf16 v[92:95], v[112:115], v[216:223], v1\n"
    "s_waitcnt lgkmcnt(0)\n"
    "v_cvt_pk_bf16_f32 v55, v110, v111\n"
    "v_smfmac_f32_16x16x64_bf16 v[96:99], v[104:107], v[224:231], v1\n"
    "v_and_b32 v125, 0xffff0000, v78\n"
    "v_smfmac_f32_16x16x64_bf16 v[96:99], v[116:119], v[232:239], v1\n"
    "v_lshlrev_b32 v126, 16, v79\n"
    "v_smfmac_f32_16x16x64_bf16 v[96:99], v[88:91], v[240:247], v1\n"
    "v_cvt_pk_bf16_f32 v46, v92, v93\n"
    "v_smfmac_f32_16x16x64_bf16 v[96:99], v[112:115], v[248:255], v1\n"
    "v_cvt_pk_bf16_f32 v47, v94, v95\n"
    "v_and_b32 v127, 0xffff0000, v79\n"
    "s_nop 3\n"
    "v_cvt_pk_bf16_f32 v38, v96, v97\n"
    "v_cvt_pk_bf16_f32 v39, v98, v99\n"
    "s_waitcnt vmcnt(0) lgkmcnt(0)\n"
    "s_cmp_eq_u32 s69, 0\n"
    "v_lshlrev_b32 v112, 16, v100\n"
    "v_and_b32 v113, 0xffff0000, v100\n"
    "s_cbranch_scc1 rwend_%=\n"
    "s_cmp_lt_u32 s57, 0x200000\n"
    "v_lshlrev_b32 v114, 16, v101\n"
    "v_and_b32 v115, 0xffff0000, v101\n"
    "s_cbranch_scc0 rwadv_%=\n"
    "v_lshlrev_b32 v116, 16, v102\n"
    "v_and_b32 v117, 0xffff0000, v102\n"
    "v_lshlrev_b32 v118, 16, v103\n"
    "v_and_b32 v119, 0xffff0000, v103\n"
    "v_pk_add_f32 v[120:121], v[120:121], v[112:113]\n"
    "v_pk_add_f32 v[122:123], v[122:123], v[114:115]\n"
    "v_pk_add_f32 v[124:125], v[124:125], v[116:117]\n"
    "v_pk_add_f32 v[126:127], v[126:127], v[118:119]\n"
    "v_cvt_pk_bf16_f32 v100, v120, v121\n"
    "v_cvt_pk_bf16_f32 v101, v122, v123\n"
    "v_cvt_pk_bf16_f32 v102, v124, v125\n"
    "v_cvt_pk_bf16_f32 v103, v126, v127\n"
    "s_cmp_eq_u32 s40, 0\n"
    "s_cbranch_scc0 rwl1_%=\n"
    "v_max3_u32 v6, v72, v73, v74\n"
    "v_max_u32 v6, v6, v75\n"
    "v_cmp_eq_u32 vcc, -1, v6\n"
    "v_lshlrev_b32 v120, 16, v100\n"
    "v_and_b32 v121, 0xffff0000, v100\n"
    "v_lshlrev_b32 v122, 16, v101\n"
    "v_and_b32 v123, 0xffff0000, v101\n"
    "v_lshlrev_b32 v124, 16, v102\n"
    "v_and_b32 v125, 0xffff0000, v102\n"
    "v_lshlrev_b32 v126, 16, v103\n"
    "v_and_b32 v127, 0xffff0000, v103\n"
    "s_cbranch_vccnz rwchk_%=\n"
    "rwok_%=:\n"
    "v_pk_add_f32 v[2:3], v[120:121], v[122:123]\n"
    "v_pk_add_f32 v[4:5], v[124:125], v[126:127]\n"
    "v_pk_add_f32 v[2:3], v[2:3], v[4:5]\n"
    "v_add_f32 v2, v2, v3\n"
    "v_lshlrev_b32 v104, 16, v72\n"
    "v_and_b32 v105, 0xffff0000, v72\n"
    "v_add_f32_dpp v2, v2, v2 row_ror:8 row_mask:0xf bank_mask:0xf\n"
    "v_lshlrev_b32 v106, 16, v73\n"
    "v_and_b32 v107, 0xffff0000, v73\n"
    "v_add_f32_dpp v2, v2, v2 row_ror:4 row_mask:0xf bank_mask:0xf\n"
    "v_lshlrev_b32 v108, 16, v74\n"
    "v_and_b32 v109, 0xffff0000, v74\n"
    "v_add_f32_dpp v2, v2, v2 row_ror:2 row_mask:0xf bank_mask:0xf\n"
    "v_lshlrev_b32 v110, 16, v75\n"
    "v_and_b32 v111, 0xffff0000, v75\n"
    "v_add_f32_dpp v2, v2, v2 row_ror:1 row_mask:0xf bank_mask:0xf\n"
    "v_mov_b32 v3, v2\n"
    "s_nop 1\n"
    "v_permlane32_swap_b32 v2, v3\n"
    "v_add_f32 v2, v2, v3\n"
    "v_mov_b32 v3, v2\n"
    "s_nop 1\n"
    "v_permlane16_swap_b32 v2, v3\n"
    "v_add_f32 v2, v2, v3\n"
    "v_mul_f32 v2, 0x3b000000, v2\n"
    "v_pk_add_f32 v[112:113], v[120:121], v[2:3] op_sel_hi:[1,0] neg_lo:[0,1] neg_hi:[0,1]\n"
    "v_pk_add_f32 v[114:115], v[122:123], v[2:3] op_sel_hi:[1,0] neg_lo:[0,1] neg_hi:[0,1]\n"
    "v_pk_add_f32 v[116:117], v[124:125], v[2:3] op_sel_hi:[1,0] neg_lo:[0,1] neg_hi:[0,1]\n"
    "v_pk_add_f32 v[118:119], v[126:127], v[2:3] op_sel_hi:[1,0] neg_lo:[0,1] neg_hi:[0,1]\n"
    "v_pk_mul_f32 v[4:5], v[112:113], v[112:113]\n"
    "v_pk_fma_f32 v[4:5], v[114:115], v[114:115], v[4:5]\n"
    "v_pk_fma_f32 v[4:5], v[116:117], v[116:117], v[4:5]\n"
    "v_pk_fma_f32 v[4:5], v[118:119], v[118:119], v[4:5]\n"
    "v_add_f32 v4, v4, v5\n"
    "v_cvt_pk_bf16_f32 v2, v2, v2\n"
    "v_and_b32 v2, 0xffff0000, v2\n"
    "v_add_f32_dpp v4, v4, v4 row_ror:8 row_mask:0xf bank_mask:0xf\n"
    "v_pk_add_f32 v[112:113], v[120:121], v[2:3] op_sel_hi:[1,0] neg_lo:[0,1] neg_hi:[0,1]\n"
    "v_pk_add_f32 v[114:115], v[122:123], v[2:3] op_sel_hi:[1,0] neg_lo:[0,1] neg_hi:[0,1]\n"
    "v_add_f32_dpp v4, v4, v4 row_ror:4 row_mask:0xf bank_mask:0xf\n"
    "v_pk_add_f32 v[116:117], v[124:125], v[2:3] op_sel_hi:[1,0] neg_lo:[0,1] neg_hi:[0,1]\n"
    "v_pk_add_f32 v[118:119], v[126:127], v[2:3] op_sel_hi:[1,0] neg_lo:[0,1] neg_hi:[0,1]\n"
    "v_add_f32_dpp v4, v4, v4 row_ror:2 row_mask:0xf bank_mask:0xf\n"
    "v_cvt_pk_bf16_f32 v100, v112, v113\n"
    "v_cvt_pk_bf16_f32 v101, v114, v115\n"
    "v_add_f32_dpp v4, v4, v4 row_ror:1 row_mask:0xf bank_mask:0xf\n"
    "v_mov_b32 v5, v4\n"
    "v_cvt_pk_bf16_f32 v102, v116, v117\n"
    "v_cvt_pk_bf16_f32 v103, v118, v119\n"
    "v_permlane32_swap_b32 v4, v5\n"
    "v_add_f32 v4, v4, v5\n"
    "v_mov_b32 v5, v4\n"
    "v_lshlrev_b32 v112, 16, v100\n"
    "v_and_b32 v113, 0xffff0000, v100\n"
    "v_permlane16_swap_b32 v4, v5\n"
    "v_add_f32 v4, v4, v5\n"
    "v_mul_f32 v4, 0x3b000000, v4\n"
    "v_cvt_pk_bf16_f32 v4, v4, v4\n"
    "v_and_b32 v4, 0xffff0000, v4\n"
    "v_add_f32 v4, 0x3727c5ac, v4\n"
    "v_cvt_pk_bf16_f32 v4, v4, v4\n"
    "v_and_b32 v4, 0xffff0000, v4\n"
    "v_sqrt_f32 v4, v4\n"
    "v_lshlrev_b32 v114, 16, v101\n"
    "v_and_b32 v115, 0xffff0000, v101\n"
    "v_cvt_pk_bf16_f32 v4, v4, v4\n"
    "v_and_b32 v4, 0xffff0000, v4\n"
    "v_rcp_f32 v4, v4\n"
    "v_lshlrev_b32 v116, 16, v102\n"
    "v_and_b32 v117, 0xffff0000, v102\n"
    "v_lshlrev_b32 v118, 16, v103\n"
    "v_and_b32 v119, 0xffff0000, v103\n"
    "v_pk_mul_f32 v[112:113], v[112:113], v[4:5] op_sel_hi:[1,0]\n"
    "v_pk_mul_f32 v[114:115], v[114:115], v[4:5] op_sel_hi:[1,0]\n"
    "v_pk_mul_f32 v[116:117], v[116:117], v[4:5] op_sel_hi:[1,0]\n"
    "v_pk_mul_f32 v[118:119], v[118:119], v[4:5] op_sel_hi:[1,0]\n"
    "v_cvt_pk_bf16_f32 v100, v112, v113\n"
    "v_cvt_pk_bf16_f32 v101, v114, v115\n"
    "v_cvt_pk_bf16_f32 v102, v116, v117\n"
    "v_cvt_pk_bf16_f32 v103, v118, v119\n"
    "v_lshlrev_b32 v112, 16, v100\n"
    "v_and_b32 v113, 0xffff0000, v100\n"
    "v_lshlrev_b32 v114, 16, v101\n"
    "v_and_b32 v115, 0xffff0000, v101\n"
    "v_lshlrev_b32 v116, 16, v102\n"
    "v_and_b32 v117, 0xffff0000, v102\n"
    "v_lshlrev_b32 v118, 16, v103\n"
    "v_and_b32 v119, 0xffff0000, v103\n"
    "v_pk_mul_f32 v[112:113], v[112:113], v[104:105]\n"
    "v_pk_mul_f32 v[114:115], v[114:115], v[106:107]\n"
    "v_pk_mul_f32 v[116:117], v[116:117], v[108:109]\n"
    "v_pk_mul_f32 v[118:119], v[118:119], v[110:111]\n"
    "v_cvt_pk_bf16_f32 v100, v112, v113\n"
    "v_cvt_pk_bf16_f32 v101, v114, v115\n"
    "v_cvt_pk_bf16_f32 v102, v116, v117\n"
    "v_cvt_pk_bf16_f32 v103, v118, v119\n"
    "v_lshlrev_b32 v112, 16, v100\n"
    "v_and_b32 v113, 0xffff0000, v100\n"
    "v_lshlrev_b32 v114, 16, v101\n"
    "v_and_b32 v115, 0xffff0000, v101\n"
    "v_lshlrev_b32 v116, 16, v102\n"
    "v_and_b32 v117, 0xffff0000, v102\n"
    "v_lshlrev_b32 v118, 16, v103\n"
    "v_and_b32 v119, 0xffff0000, v103\n"
    "v_pk_add_f32 v[120:121], v[120:121], v[112:113]\n"
    "v_pk_add_f32 v[122:123], v[122:123], v[114:115]\n"
    "v_pk_add_f32 v[124:125], v[124:125], v[116:117]\n"
    "v_pk_add_f32 v[126:127], v[126:127], v[118:119]\n"
    "v_cvt_pk_bf16_f32 v96, v120, v121\n"
    "v_cvt_pk_bf16_f32 v97, v122, v123\n"
    "v_cvt_pk_bf16_f32 v98, v124, v125\n"
    "v_cvt_pk_bf16_f32 v99, v126, v127\n"
    "v_mov_b64 v[112:113], -1\n"
    "v_mov_b64 v[114:115], -1\n"
    "s_add_u32 s62, s57, s36\n"
    "v_mfma_f32_16x16x16_bf16 v[104:107], v[72:73], v[72:73], 0\n"
    "buffer_store_dwordx4 v[96:99], v0, s[12:15], s62 offen sc1\n"
    "s_cmp_lt_u32 s57, 0x1e0000\n"
    "s_cselect_b32 s62, 0x10000, 0\n"
    "s_add_u32 s62, s62, s58\n"
    "v_mfma_f32_16x16x16_bf16 v[104:107], v[72:73], v[72:73], 0\n"
    "buffer_store_dwordx4 v[112:115], v8, s[12:15], s62 offen sc1\n"
    "v_lshlrev_b32 v120, 16, v96\n"
    "v_and_b32 v121, 0xffff0000, v96\n"
    "v_lshlrev_b32 v122, 16, v97\n"
    "v_and_b32 v123, 0xffff0000, v97\n"
    "v_lshlrev_b32 v124, 16, v98\n"
    "v_and_b32 v125, 0xffff0000, v98\n"
    "v_lshlrev_b32 v126, 16, v99\n"
    "v_and_b32 v127, 0xffff0000, v99\n"
    "v_pk_add_f32 v[2:3], v[120:121], v[122:123]\n"
    "v_pk_add_f32 v[4:5], v[124:125], v[126:127]\n"
    "v_pk_add_f32 v[2:3], v[2:3], v[4:5]\n"
    "v_add_f32 v2, v2, v3\n"
    "v_add_u32 v6, s42, 1\n"
    "v_lshlrev_b32 v9, 20, v0\n"
    "v_add_f32_dpp v2, v2, v2 row_ror:8 row_mask:0xf bank_mask:0xf\n"
    "v_and_b32 v9, 0x20000000, v9\n"
    "v_lshl_or_b32 v9, v0, 3, v9\n"
    "v_add_f32_dpp v2, v2, v2 row_ror:4 row_mask:0xf bank_mask:0xf\n"
    "s_nop 1\n"
    "v_add_f32_dpp v2, v2, v2 row_ror:2 row_mask:0xf bank_mask:0xf\n"
    "s_nop 1\n"
    "v_add_f32_dpp v2, v2, v2 row_ror:1 row_mask:0xf bank_mask:0xf\n"
    "v_mov_b32 v3, v2\n"
    "s_nop 1\n"
    "v_permlane32_swap_b32 v2, v3\n"
    "v_add_f32 v2, v2, v3\n"
    "v_mov_b32 v3, v2\n"
    "s_nop 1\n"
    "v_permlane16_swap_b32 v2, v3\n"
    "v_add_f32 v2, v2, v3\n"
    "v_mul_f32 v2, 0x3b000000, v2\n"
    "v_pk_add_f32 v[112:113], v[120:121], v[2:3] op_sel_hi:[1,0] neg_lo:[0,1] neg_hi:[0,1]\n"
    "v_pk_add_f32 v[114:115], v[122:123], v[2:3] op_sel_hi:[1,0] neg_lo:[0,1] neg_hi:[0,1]\n"
    "v_pk_add_f32 v[116:117], v[124:125], v[2:3] op_sel_hi:[1,0] neg_lo:[0,1] neg_hi:[0,1]\n"
    "v_pk_add_f32 v[118:119], v[126:127], v[2:3] op_sel_hi:[1,0] neg_lo:[0,1] neg_hi:[0,1]\n"
    "v_pk_mul_f32 v[4:5], v[112:113], v[112:113]\n"
    "v_pk_fma_f32 v[4:5], v[114:115], v[114:115], v[4:5]\n"
    "v_pk_fma_f32 v[4:5], v[116:117], v[116:117], v[4:5]\n"
    "v_pk_fma_f32 v[4:5], v[118:119], v[118:119], v[4:5]\n"
    "v_add_f32 v4, v4, v5\n"
    "v_cvt_pk_bf16_f32 v2, v2, v2\n"
    "v_and_b32 v2, 0xffff0000, v2\n"
    "v_add_f32_dpp v4, v4, v4 row_ror:8 row_mask:0xf bank_mask:0xf\n"
    "v_pk_add_f32 v[112:113], v[120:121], v[2:3] op_sel_hi:[1,0] neg_lo:[0,1] neg_hi:[0,1]\n"
    "v_pk_add_f32 v[114:115], v[122:123], v[2:3] op_sel_hi:[1,0] neg_lo:[0,1] neg_hi:[0,1]\n"
    "v_add_f32_dpp v4, v4, v4 row_ror:4 row_mask:0xf bank_mask:0xf\n"
    "v_pk_add_f32 v[116:117], v[124:125], v[2:3] op_sel_hi:[1,0] neg_lo:[0,1] neg_hi:[0,1]\n"
    "v_pk_add_f32 v[118:119], v[126:127], v[2:3] op_sel_hi:[1,0] neg_lo:[0,1] neg_hi:[0,1]\n"
    "v_add_f32_dpp v4, v4, v4 row_ror:2 row_mask:0xf bank_mask:0xf\n"
    "v_cvt_pk_bf16_f32 v100, v112, v113\n"
    "v_cvt_pk_bf16_f32 v101, v114, v115\n"
    "v_add_f32_dpp v4, v4, v4 row_ror:1 row_mask:0xf bank_mask:0xf\n"
    "v_mov_b32 v5, v4\n"
    "v_cvt_pk_bf16_f32 v102, v116, v117\n"
    "v_cvt_pk_bf16_f32 v103, v118, v119\n"
    "v_permlane32_swap_b32 v4, v5\n"
    "v_add_f32 v4, v4, v5\n"
    "v_mov_b32 v5, v4\n"
    "v_lshlrev_b32 v112, 16, v100\n"
    "v_and_b32 v113, 0xffff0000, v100\n"
    "v_permlane16_swap_b32 v4, v5\n"
    "v_add_f32 v4, v4, v5\n"
    "v_mul_f32 v4, 0x3b000000, v4\n"
    "v_cvt_pk_bf16_f32 v4, v4, v4\n"
    "v_and_b32 v4, 0xffff0000, v4\n"
    "v_add_f32 v4, 0x3727c5ac, v4\n"
    "v_cvt_pk_bf16_f32 v4, v4, v4\n"
    "v_and_b32 v4, 0xffff0000, v4\n"
    "v_sqrt_f32 v4, v4\n"
    "v_lshlrev_b32 v114, 16, v101\n"
    "v_and_b32 v115, 0xffff0000, v101\n"
    "v_cvt_pk_bf16_f32 v4, v4, v4\n"
    "v_and_b32 v4, 0xffff0000, v4\n"
    "v_rcp_f32 v4, v4\n"
    "v_lshlrev_b32 v116, 16, v102\n"
    "v_and_b32 v117, 0xffff0000, v102\n"
    "v_lshlrev_b32 v118, 16, v103\n"
    "v_and_b32 v119, 0xffff0000, v103\n"
    "v_pk_mul_f32 v[112:113], v[112:113], v[4:5] op_sel_hi:[1,0]\n"
    "v_pk_mul_f32 v[114:115], v[114:115], v[4:5] op_sel_hi:[1,0]\n"
    "v_pk_mul_f32 v[116:117], v[116:117], v[4:5] op_sel_hi:[1,0]\n"
    "v_pk_mul_f32 v[118:119], v[118:119], v[4:5] op_sel_hi:[1,0]\n"
    "v_cvt_pk_bf16_f32 v100, v112, v113\n"
    "v_cvt_pk_bf16_f32 v101, v114, v115\n"
    "v_cvt_pk_bf16_f32 v102, v116, v117\n"
    "v_cvt_pk_bf16_f32 v103, v118, v119\n"
    "v_min_u32 v100, -2, v100\n"
    "v_min_u32 v101, -2, v101\n"
    "v_min_u32 v102, -2, v102\n"
    "v_min_u32 v103, -2, v103\n"
    "s_waitcnt vmcnt(0)\n"
    "buffer_store_dwordx4 v[100:103], v8, s[12:15], s58 offen sc1\n"
    "s_memrealtime s[80:81]\n"
    "s_mul_i32 s61, s71, 4\n"
    "s_add_u32 s61, s61, 0xffffff90\n"
    "s_sub_u32 s62, 4, s42\n"
    "s_max_i32 s62, s62, 0\n"
    "s_min_i32 s62, s62, 4\n"
    "s_mul_i32 s62, s62, 0\n"
    "s_add_u32 s61, s61, s62\n"
    "s_waitcnt lgkmcnt(0)\n"
    "s_add_u32 s61, s61, s80\n"
    "v_mov_b32 v7, s61\n"
    "buffer_store_dwordx2 v[6:7], v9, s[16:19], s70 offen sc1\n"
    "s_nop 1\n"
    "rwadv_%=:\n"
    "s_add_u32 s58, s58, 0x10000\n"
    "s_add_u32 s57, s57, 0x20000\n"
    "s_add_u32 s42, s42, 4\n"
    "s_xor_b32 s46, s46, 0x10000\n"
    "s_xor_b32 s34, s34, 0x10000\n"
    "s_xor_b32 s47, s47, 0x10000\n"
    "rwend_%=:\n"
    "s_cmp_eq_u32 s99, 0\n"
    "s_cbranch_scc1 yck_%=\n"
    "yok_%=:\n"
    "s_branch turn_%=\n"
    "epi_%=:\n"
    "s_nop 7\n"
    "ds_read_b128 v[100:103], v0 offset:29760\n"
    "ds_read_b128 v[124:127], v0 offset:27712\n"
    "s_cmp_lg_u32 s69, 0\n"
    "s_cselect_b32 s62, s57, -1\n"
    "s_cmp_lt_u32 s62, 0x200000\n"
    "s_cselect_b32 s6, 0x200000, 0\n"
    "s_cselect_b32 s14, 0x3400000, 0\n"
    "s_cselect_b32 s62, s57, 0\n"
    "s_cmp_eq_u32 s40, 0\n"
    "s_cselect_b32 s14, s14, 0\n"
    "buffer_load_dwordx4 v[108:111], v0, s[4:7], s62 offen sc1\n"
    "v_lshlrev_b32 v2, 5, v0\n"
    "s_add_u32 s62, s57, s3\n"
    "v_lshrrev_b32 v2, s99, v2\n"
    "buffer_load_dwordx2 v[96:97], v2, s[12:15], s62 offen sc1\n"
    "s_add_u32 s62, s62, s98\n"
    "buffer_load_dwordx2 v[98:99], v2, s[12:15], s62 offen offset:256 sc1\n"
    "v_lshlrev_b32 v4, 3, v0\n"
    "v_and_b32 v4, 0x380, v4\n"
    "s_lshl_b32 s61, s41, 29\n"
    "s_waitcnt lgkmcnt(1)\n"
    "v_or_b32 v5, s61, v101\n"
    "s_sub_u32 s61, s65, s0\n"
    "s_max_i32 s61, s61, 0\n"
    "s_min_i32 s61, s61, 34\n"
    "s_and_b32 s1, s97, 0x100\n"
    "s_or_b32 s61, s61, s1\n"
    "s_pack_ll_b32_b16 s61, s33, s61\n"
    "v_mov_b32 v2, s61\n"
    "s_mov_b32 s1, s80\n"
    "v_mov_b32 v3, s1\n"
    "s_waitcnt vmcnt(0) lgkmcnt(0)\n"
    "s_nop 7\n"
    "s_barrier\n"
    "buffer_store_dwordx2 v[2:3], v5, s[8:11], s53 offen sc1\n"
    "s_sleep 5\n"
    "s_cmp_eq_u32 s42, 63\n"
    "s_cbranch_scc1 epiF_%=\n"
    "s_sleep 14\n"
    "epiF_%=:\n"
    "buffer_load_dwordx2 v[2:3], v4, s[8:11], s54 offen sc1\n"
    "s_waitcnt vmcnt(0)\n"
    "v_cmp_ne_u16 vcc, s33, v2\n"
    "s_cbranch_vccz epiok_%=\n"
    "s_cmp_eq_u32 s44, 0\n"
    "s_cbranch_scc1 epiok_%=\n"
    "s_sub_u32 s44, s44, 1\n"
    "s_branch epiF_%=\n"
    "epiok_%=:\n"
    "s_waitcnt vmcnt(0) lgkmcnt(0)\n"
    "s_nop 7\n"
    "s_barrier\n"
    "buffer_load_dwordx4 v[120:123], v0, s[8:11], s56 offen sc1\n"
    "buffer_load_dwordx4 v[116:119], v0, s[8:11], s56 offen offset:1024 sc1\n"
    "buffer_load_dwordx4 v[112:115], v0, s[8:11], s56 offen offset:2048 sc1\n"
    "buffer_load_dwordx4 v[104:107], v0, s[8:11], s56 offen offset:3072 sc1\n"
    "buffer_load_dwordx4 v[92:95], v0, s[8:11], s35 offen sc1\n"
    "buffer_load_dwordx4 v[88:91], v0, s[8:11], s35 offen offset:1024 sc1\n"
    "buffer_load_dwordx4 v[84:87], v0, s[8:11], s35 offen offset:2048 sc1\n"
    "buffer_load_dwordx4 v[80:83], v0, s[8:11], s35 offen offset:3072 sc1\n"
    "v_bfe_u32 v2, v0, 8, 1\n"
    "v_lshlrev_b32 v2, 9, v2\n"
    "v_bfe_u32 v76, v0, 2, 6\n"
    "v_add3_u32 v2, v2, s68, v76\n"
    "s_waitcnt vmcnt(7)\n"
    "v_mfma_f32_16x16x32_bf16 v[72:75], v[124:127], v[120:123], 0\n"
    "s_waitcnt vmcnt(6)\n"
    "v_mfma_f32_16x16x32_bf16 v[72:75], v[124:127], v[116:119], v[72:75]\n"
    "s_waitcnt vmcnt(5)\n"
    "v_mfma_f32_16x16x32_bf16 v[72:75], v[124:127], v[112:115], v[72:75]\n"
    "s_waitcnt vmcnt(4)\n"
    "v_mfma_f32_16x16x32_bf16 v[72:75], v[124:127], v[104:107], v[72:75]\n"
    "s_waitcnt vmcnt(3)\n"
    "v_mfma_f32_16x16x32_bf16 v[72:75], v[124:127], v[92:95], v[72:75]\n"
    "s_waitcnt vmcnt(2)\n"
    "v_mfma_f32_16x16x32_bf16 v[72:75], v[124:127], v[88:91], v[72:75]\n"
    "s_waitcnt vmcnt(1)\n"
    "v_mfma_f32_16x16x32_bf16 v[72:75], v[124:127], v[84:87], v[72:75]\n"
    "s_waitcnt vmcnt(0)\n"
    "v_mfma_f32_16x16x32_bf16 v[72:75], v[124:127], v[80:83], v[72:75]\n"
    "s_nop 3\n"
    "v_mov_b32 v0, v0\n"
    "s_nop 2\n"
    "v_cvt_pk_bf16_f32 v72, v72, v73\n"
    "v_cvt_pk_bf16_f32 v73, v74, v75\n"
    "ds_write2st64_b32 v2, v72, v73 offset0:121 offset1:122\n"
    "s_waitcnt lgkmcnt(0)\n"
    "s_barrier\n"
    "ds_read_b128 v[76:79], v0 offset:30976\n"
    "s_waitcnt vmcnt(0) lgkmcnt(0)\n"
    "s_cmp_eq_u32 s69, 0\n"
    "v_lshlrev_b32 v120, 16, v76\n"
    "v_and_b32 v121, 0xffff0000, v76\n"
    "s_cbranch_scc1 reend_%=\n"
    "s_cmp_lt_u32 s57, 0x200000\n"
    "v_lshlrev_b32 v122, 16, v77\n"
    "v_and_b32 v123, 0xffff0000, v77\n"
    "s_cbranch_scc0 readv_%=\n"
    "v_lshlrev_b32 v124, 16, v78\n"
    "v_and_b32 v125, 0xffff0000, v78\n"
    "v_lshlrev_b32 v126, 16, v79\n"
    "v_and_b32 v127, 0xffff0000, v79\n"
    "v_lshlrev_b32 v112, 16, v108\n"
    "v_and_b32 v113, 0xffff0000, v108\n"
    "v_lshlrev_b32 v114, 16, v109\n"
    "v_and_b32 v115, 0xffff0000, v109\n"
    "v_lshlrev_b32 v116, 16, v110\n"
    "v_and_b32 v117, 0xffff0000, v110\n"
    "v_lshlrev_b32 v118, 16, v111\n"
    "v_and_b32 v119, 0xffff0000, v111\n"
    "v_pk_add_f32 v[112:113], v[112:113], v[120:121]\n"
    "v_pk_add_f32 v[114:115], v[114:115], v[122:123]\n"
    "v_pk_add_f32 v[116:117], v[116:117], v[124:125]\n"
    "v_pk_add_f32 v[118:119], v[118:119], v[126:127]\n"
    "v_cvt_pk_bf16_f32 v92, v112, v113\n"
    "v_cvt_pk_bf16_f32 v93, v114, v115\n"
    "v_cvt_pk_bf16_f32 v94, v116, v117\n"
    "v_cvt_pk_bf16_f32 v95, v118, v119\n"
    "s_cmp_eq_u32 s40, 0\n"
    "s_cbranch_scc0 rel1_%=\n"
    "v_max3_u32 v6, v96, v97, v98\n"
    "v_max_u32 v6, v6, v99\n"
    "v_cmp_eq_u32 vcc, -1, v6\n"
    "v_lshlrev_b32 v112, 16, v92\n"
    "v_and_b32 v113, 0xffff0000, v92\n"
    "v_lshlrev_b32 v114, 16, v93\n"
    "v_and_b32 v115, 0xffff0000, v93\n"
    "v_lshlrev_b32 v116, 16, v94\n"
    "v_and_b32 v117, 0xffff0000, v94\n"
    "v_lshlrev_b32 v118, 16, v95\n"
    "v_and_b32 v119, 0xffff0000, v95\n"
    "s_cbranch_vccnz rechk_%=\n"
    "reok_%=:\n"
    "v_pk_add_f32 v[2:3], v[112:113], v[114:115]\n"
    "v_pk_add_f32 v[4:5], v[116:117], v[118:119]\n"
    "v_pk_add_f32 v[2:3], v[2:3], v[4:5]\n"
    "v_add_f32 v2, v2, v3\n"
    "v_lshlrev_b32 v104, 16, v96\n"
    "v_and_b32 v105, 0xffff0000, v96\n"
    "v_add_f32_dpp v2, v2, v2 row_ror:8 row_mask:0xf bank_mask:0xf\n"
    "v_lshlrev_b32 v106, 16, v97\n"
    "v_and_b32 v107, 0xffff0000, v97\n"
    "v_add_f32_dpp v2, v2, v2 row_ror:4 row_mask:0xf bank_mask:0xf\n"
    "v_lshlrev_b32 v108, 16, v98\n"
    "v_and_b32 v109, 0xffff0000, v98\n"
    "v_add_f32_dpp v2, v2, v2 row_ror:2 row_mask:0xf bank_mask:0xf\n"
    "v_lshlrev_b32 v110, 16, v99\n"
    "v_and_b32 v111, 0xffff0000, v99\n"
    "v_add_f32_dpp v2, v2, v2 row_ror:1 row_mask:0xf bank_mask:0xf\n"
    "v_mov_b32 v3, v2\n"
    "s_nop 1\n"
    "v_permlane32_swap_b32 v2, v3\n"
    "v_add_f32 v2, v2, v3\n"
    "v_mov_b32 v3, v2\n"
    "s_nop 1\n"
    "v_permlane16_swap_b32 v2, v3\n"
    "v_add_f32 v2, v2, v3\n"
    "v_mul_f32 v2, 0x3b000000, v2\n"
    "v_pk_add_f32 v[120:121], v[112:113], v[2:3] op_sel_hi:[1,0] neg_lo:[0,1] neg_hi:[0,1]\n"
    "v_pk_add_f32 v[122:123], v[114:115], v[2:3] op_sel_hi:[1,0] neg_lo:[0,1] neg_hi:[0,1]\n"
    "v_pk_add_f32 v[124:125], v[116:117], v[2:3] op_sel_hi:[1,0] neg_lo:[0,1] neg_hi:[0,1]\n"
    "v_pk_add_f32 v[126:127], v[118:119], v[2:3] op_sel_hi:[1,0] neg_lo:[0,1] neg_hi:[0,1]\n"
    "v_pk_mul_f32 v[4:5], v[120:121], v[120:121]\n"
    "v_pk_fma_f32 v[4:5], v[122:123], v[122:123], v[4:5]\n"
    "v_pk_fma_f32 v[4:5], v[124:125], v[124:125], v[4:5]\n"
    "v_pk_fma_f32 v[4:5], v[126:127], v[126:127], v[4:5]\n"
    "v_add_f32 v4, v4, v5\n"
    "v_cvt_pk_bf16_f32 v2, v2, v2\n"
    "v_and_b32 v2, 0xffff0000, v2\n"
    "v_add_f32_dpp v4, v4, v4 row_ror:8 row_mask:0xf bank_mask:0xf\n"
    "v_pk_add_f32 v[120:121], v[112:113], v[2:3] op_sel_hi:[1,0] neg_lo:[0,1] neg_hi:[0,1]\n"
    "v_pk_add_f32 v[122:123], v[114:115], v[2:3] op_sel_hi:[1,0] neg_lo:[0,1] neg_hi:[0,1]\n"
    "v_add_f32_dpp v4, v4, v4 row_ror:4 row_mask:0xf bank_mask:0xf\n"
    "v_pk_add_f32 v[124:125], v[116:117], v[2:3] op_sel_hi:[1,0] neg_lo:[0,1] neg_hi:[0,1]\n"
    "v_pk_add_f32 v[126:127], v[118:119], v[2:3] op_sel_hi:[1,0] neg_lo:[0,1] neg_hi:[0,1]\n"
    "v_add_f32_dpp v4, v4, v4 row_ror:2 row_mask:0xf bank_mask:0xf\n"
    "v_cvt_pk_bf16_f32 v92, v120, v121\n"
    "v_cvt_pk_bf16_f32 v93, v122, v123\n"
    "v_add_f32_dpp v4, v4, v4 row_ror:1 row_mask:0xf bank_mask:0xf\n"
    "v_mov_b32 v5, v4\n"
    "v_cvt_pk_bf16_f32 v94, v124, v125\n"
    "v_cvt_pk_bf16_f32 v95, v126, v127\n"
    "v_permlane32_swap_b32 v4, v5\n"
    "v_add_f32 v4, v4, v5\n"
    "v_mov_b32 v5, v4\n"
    "v_lshlrev_b32 v120, 16, v92\n"
    "v_and_b32 v121, 0xffff0000, v92\n"
    "v_permlane16_swap_b32 v4, v5\n"
    "v_add_f32 v4, v4, v5\n"
    "v_mul_f32 v4, 0x3b000000, v4\n"
    "v_cvt_pk_bf16_f32 v4, v4, v4\n"
    "v_and_b32 v4, 0xffff0000, v4\n"
    "v_add_f32 v4, 0x3727c5ac, v4\n"
    "v_cvt_pk_bf16_f32 v4, v4, v4\n"
    "v_and_b32 v4, 0xffff0000, v4\n"
    "v_sqrt_f32 v4, v4\n"
    "v_lshlrev_b32 v122, 16, v93\n"
    "v_and_b32 v123, 0xffff0000, v93\n"
    "v_cvt_pk_bf16_f32 v4, v4, v4\n"
    "v_and_b32 v4, 0xffff0000, v4\n"
    "v_rcp_f32 v4, v4\n"
    "v_lshlrev_b32 v124, 16, v94\n"
    "v_and_b32 v125, 0xffff0000, v94\n"
    "v_lshlrev_b32 v126, 16, v95\n"
    "v_and_b32 v127, 0xffff0000, v95\n"
    "v_pk_mul_f32 v[120:121], v[120:121], v[4:5] op_sel_hi:[1,0]\n"
    "v_pk_mul_f32 v[122:123], v[122:123], v[4:5] op_sel_hi:[1,0]\n"
    "v_pk_mul_f32 v[124:125], v[124:125], v[4:5] op_sel_hi:[1,0]\n"
    "v_pk_mul_f32 v[126:127], v[126:127], v[4:5] op_sel_hi:[1,0]\n"
    "v_cvt_pk_bf16_f32 v92, v120, v121\n"
    "v_cvt_pk_bf16_f32 v93, v122, v123\n"
    "v_cvt_pk_bf16_f32 v94, v124, v125\n"
    "v_cvt_pk_bf16_f32 v95, v126, v127\n"
    "v_lshlrev_b32 v120, 16, v92\n"
    "v_and_b32 v121, 0xffff0000, v92\n"
    "v_lshlrev_b32 v122, 16, v93\n"
    "v_and_b32 v123, 0xffff0000, v93\n"
    "v_lshlrev_b32 v124, 16, v94\n"
    "v_and_b32 v125, 0xffff0000, v94\n"
    "v_lshlrev_b32 v126, 16, v95\n"
    "v_and_b32 v127, 0xffff0000, v95\n"
    "v_pk_mul_f32 v[120:121], v[120:121], v[104:105]\n"
    "v_pk_mul_f32 v[122:123], v[122:123], v[106:107]\n"
    "v_pk_mul_f32 v[124:125], v[124:125], v[108:109]\n"
    "v_pk_mul_f32 v[126:127], v[126:127], v[110:111]\n"
    "v_cvt_pk_bf16_f32 v92, v120, v121\n"
    "v_cvt_pk_bf16_f32 v93, v122, v123\n"
    "v_cvt_pk_bf16_f32 v94, v124, v125\n"
    "v_cvt_pk_bf16_f32 v95, v126, v127\n"
    "v_lshlrev_b32 v120, 16, v92\n"
    "v_and_b32 v121, 0xffff0000, v92\n"
    "v_lshlrev_b32 v122, 16, v93\n"
    "v_and_b32 v123, 0xffff0000, v93\n"
    "v_lshlrev_b32 v124, 16, v94\n"
    "v_and_b32 v125, 0xffff0000, v94\n"
    "v_lshlrev_b32 v126, 16, v95\n"
    "v_and_b32 v127, 0xffff0000, v95\n"
    "v_pk_add_f32 v[112:113], v[112:113], v[120:121]\n"
    "v_pk_add_f32 v[114:115], v[114:115], v[122:123]\n"
    "v_pk_add_f32 v[116:117], v[116:117], v[124:125]\n"
    "v_pk_add_f32 v[118:119], v[118:119], v[126:127]\n"
    "v_cvt_pk_bf16_f32 v88, v112, v113\n"
    "v_cvt_pk_bf16_f32 v89, v114, v115\n"
    "v_cvt_pk_bf16_f32 v90, v116, v117\n"
    "v_cvt_pk_bf16_f32 v91, v118, v119\n"
    "s_add_u32 s62, s57, s36\n"
    "v_mfma_f32_16x16x16_bf16 v[104:107], v[96:97], v[96:97], 0\n"
    "buffer_store_dwordx4 v[88:91], v0, s[12:15], s62 offen sc1\n"
    "v_lshlrev_b32 v112, 16, v88\n"
    "v_and_b32 v113, 0xffff0000, v88\n"
    "v_lshlrev_b32 v114, 16, v89\n"
    "v_and_b32 v115, 0xffff0000, v89\n"
    "v_lshlrev_b32 v116, 16, v90\n"
    "v_and_b32 v117, 0xffff0000, v90\n"
    "v_lshlrev_b32 v118, 16, v91\n"
    "v_and_b32 v119, 0xffff0000, v91\n"
    "v_pk_add_f32 v[2:3], v[112:113], v[114:115]\n"
    "v_pk_add_f32 v[4:5], v[116:117], v[118:119]\n"
    "v_pk_add_f32 v[2:3], v[2:3], v[4:5]\n"
    "v_add_f32 v2, v2, v3\n"
    "v_add_u32 v6, s42, 1\n"
    "v_lshlrev_b32 v8, 20, v0\n"
    "v_add_f32_dpp v2, v2, v2 row_ror:8 row_mask:0xf bank_mask:0xf\n"
    "v_and_b32 v8, 0x20000000, v8\n"
    "v_lshl_or_b32 v8, v0, 3, v8\n"
    "v_add_f32_dpp v2, v2, v2 row_ror:4 row_mask:0xf bank_mask:0xf\n"
    "s_nop 1\n"
    "v_add_f32_dpp v2, v2, v2 row_ror:2 row_mask:0xf bank_mask:0xf\n"
    "s_nop 1\n"
    "v_add_f32_dpp v2, v2, v2 row_ror:1 row_mask:0xf bank_mask:0xf\n"
    "v_mov_b32 v3, v2\n"
    "s_nop 1\n"
    "v_permlane32_swap_b32 v2, v3\n"
    "v_add_f32 v2, v2, v3\n"
    "v_mov_b32 v3, v2\n"
    "s_nop 1\n"
    "v_permlane16_swap_b32 v2, v3\n"
    "v_add_f32 v2, v2, v3\n"
    "v_mul_f32 v2, 0x3b000000, v2\n"
    "v_pk_add_f32 v[120:121], v[112:113], v[2:3] op_sel_hi:[1,0] neg_lo:[0,1] neg_hi:[0,1]\n"
    "v_pk_add_f32 v[122:123], v[114:115], v[2:3] op_sel_hi:[1,0] neg_lo:[0,1] neg_hi:[0,1]\n"
    "v_pk_add_f32 v[124:125], v[116:117], v[2:3] op_sel_hi:[1,0] neg_lo:[0,1] neg_hi:[0,1]\n"
    "v_pk_add_f32 v[126:127], v[118:119], v[2:3] op_sel_hi:[1,0] neg_lo:[0,1] neg_hi:[0,1]\n"
    "v_pk_mul_f32 v[4:5], v[120:121], v[120:121]\n"
    "v_pk_fma_f32 v[4:5], v[122:123], v[122:123], v[4:5]\n"
    "v_pk_fma_f32 v[4:5], v[124:125], v[124:125], v[4:5]\n"
    "v_pk_fma_f32 v[4:5], v[126:127], v[126:127], v[4:5]\n"
    "v_add_f32 v4, v4, v5\n"
    "v_cvt_pk_bf16_f32 v2, v2, v2\n"
    "v_and_b32 v2, 0xffff0000, v2\n"
    "v_add_f32_dpp v4, v4, v4 row_ror:8 row_mask:0xf bank_mask:0xf\n"
    "v_pk_add_f32 v[120:121], v[112:113], v[2:3] op_sel_hi:[1,0] neg_lo:[0,1] neg_hi:[0,1]\n"
    "v_pk_add_f32 v[122:123], v[114:115], v[2:3] op_sel_hi:[1,0] neg_lo:[0,1] neg_hi:[0,1]\n"
    "v_add_f32_dpp v4, v4, v4 row_ror:4 row_mask:0xf bank_mask:0xf\n"
    "v_pk_add_f32 v[124:125], v[116:117], v[2:3] op_sel_hi:[1,0] neg_lo:[0,1] neg_hi:[0,1]\n"
    "v_pk_add_f32 v[126:127], v[118:119], v[2:3] op_sel_hi:[1,0] neg_lo:[0,1] neg_hi:[0,1]\n"
    "v_add_f32_dpp v4, v4, v4 row_ror:2 row_mask:0xf bank_mask:0xf\n"
    "v_cvt_pk_bf16_f32 v92, v120, v121\n"
    "v_cvt_pk_bf16_f32 v93, v122, v123\n"
    "v_add_f32_dpp v4, v4, v4 row_ror:1 row_mask:0xf bank_mask:0xf\n"
    "v_mov_b32 v5, v4\n"
    "v_cvt_pk_bf16_f32 v94, v124, v125\n"
    "v_cvt_pk_bf16_f32 v95, v126, v127\n"
    "v_permlane32_swap_b32 v4, v5\n"
    "v_add_f32 v4, v4, v5\n"
    "v_mov_b32 v5, v4\n"
    "v_lshlrev_b32 v120, 16, v92\n"
    "v_and_b32 v121, 0xffff0000, v92\n"
    "v_permlane16_swap_b32 v4, v5\n"
    "v_add_f32 v4, v4, v5\n"
    "v_mul_f32 v4, 0x3b000000, v4\n"
    "v_cvt_pk_bf16_f32 v4, v4, v4\n"
    "v_and_b32 v4, 0xffff0000, v4\n"
    "v_add_f32 v4, 0x3727c5ac, v4\n"
    "v_cvt_pk_bf16_f32 v4, v4, v4\n"
    "v_and_b32 v4, 0xffff0000, v4\n"
    "v_sqrt_f32 v4, v4\n"
    "v_lshlrev_b32 v122, 16, v93\n"
    "v_and_b32 v123, 0xffff0000, v93\n"
    "v_cvt_pk_bf16_f32 v4, v4, v4\n"
    "v_and_b32 v4, 0xffff0000, v4\n"
    "v_rcp_f32 v4, v4\n"
    "v_lshlrev_b32 v124, 16, v94\n"
    "v_and_b32 v125, 0xffff0000, v94\n"
    "v_lshlrev_b32 v126, 16, v95\n"
    "v_and_b32 v127, 0xffff0000, v95\n"
    "v_pk_mul_f32 v[120:121], v[120:121], v[4:5] op_sel_hi:[1,0]\n"
    "v_pk_mul_f32 v[122:123], v[122:123], v[4:5] op_sel_hi:[1,0]\n"
    "v_pk_mul_f32 v[124:125], v[124:125], v[4:5] op_sel_hi:[1,0]\n"
    "v_pk_mul_f32 v[126:127], v[126:127], v[4:5] op_sel_hi:[1,0]\n"
    "v_cvt_pk_bf16_f32 v92, v120, v121\n"
    "v_cvt_pk_bf16_f32 v93, v122, v123\n"
    "v_cvt_pk_bf16_f32 v94, v124, v125\n"
    "v_cvt_pk_bf16_f32 v95, v126, v127\n"
    "v_min_u32 v92, -2, v92\n"
    "v_min_u32 v93, -2, v93\n"
    "v_min_u32 v94, -2, v94\n"
    "v_min_u32 v95, -2, v95\n"
    "s_waitcnt vmcnt(0)\n"
    "buffer_store_dwordx4 v[92:95], v103, s[12:15], s58 offen sc1\n"
    "s_memrealtime s[80:81]\n"
    "s_mul_i32 s61, s71, 4\n"
    "s_add_u32 s61, s61, 0xffffff90\n"
    "s_sub_u32 s62, 4, s42\n"
    "s_max_i32 s62, s62, 0\n"
    "s_min_i32 s62, s62, 4\n"
    "s_mul_i32 s62, s62, 0\n"
    "s_add_u32 s61, s61, s62\n"
    "s_waitcnt lgkmcnt(0)\n"
    "s_add_u32 s61, s61, s80\n"
    "v_mov_b32 v7, s61\n"
    "buffer_store_dwordx2 v[6:7], v8, s[16:19], s70 offen sc1\n"
    "s_nop 1\n"
    "readv_%=:\n"
    "s_add_u32 s58, s58, 0x10000\n"
    "s_add_u32 s57, s57, 0x20000\n"
    "s_add_u32 s42, s42, 4\n"
    "s_xor_b32 s46, s46, 0x10000\n"
    "s_xor_b32 s34, s34, 0x10000\n"
    "s_xor_b32 s47, s47, 0x10000\n"
    "reend_%=:\n"
    "s_branch end_%=\n"
    "s_nop 1\n"
    "v_mov_b32 v0, v0\n"
    "s_nop 0\n"
    "v_mov_b32 v0, v0\n"
    "s_nop 1\n"
    "v_mov_b32 v0, v0\n"
    "s_nop 0\n"
    "v_mov_b32 v0, v0\n"
    "s_nop 2\n"
    "v_mov_b32 v0, v0\n"
    "s_nop 0\n"
    "v_mov_b32 v0, v0\n"
    "s_nop 1\n"
    "v_mov_b32 v0, v0\n"
    "s_nop 0\n"
    "v_mov_b32 v0, v0\n"
    "s_nop 1\n"
    "v_mov_b32 v0, v0\n"
    "s_nop 0\n"
    "v_mov_b32 v0, v0\n"
    "s_nop 2\n"
    "v_mov_b32 v0, v0\n"
    "s_nop 0\n"
    "v_mov_b32 v0, v0\n"
    "s_nop 1\n"
    "v_mov_b32 v0, v0\n"
    "s_nop 0\n"
    "v_mov_b32 v0, v0\n"
    "s_nop 1\n"
    "v_mov_b32 v0, v0\n"
    "s_nop 0\n"
    "v_mov_b32 v0, v0\n"
    "s_nop 2\n"
    "v_mov_b32 v0, v0\n"
    "s_nop 0\n"
    "v_mov_b32 v0, v0\n"
    "s_nop 1\n"
    "v_mov_b32 v0, v0\n"
    "s_nop 0\n"
    "v_mov_b32 v0, v0\n"
    "s_nop 1\n"
    "v_mov_b32 v0, v0\n"
    "s_nop 0\n"
    "v_mov_b32 v0, v0\n"
    "s_nop 2\n"
    "v_mov_b32 v0, v0\n"
    "s_nop 0\n"
    "v_mov_b32 v0, v0\n"
    "s_nop 1\n"
    "v_mov_b32 v0, v0\n"
    "s_nop 0\n"
    "v_mov_b32 v0, v0\n"
    "s_nop 1\n"
    "v_mov_b32 v0, v0\n"
    "s_nop 0\n"
    "v_mov_b32 v0, v0\n"
    "s_nop 2\n"
    "v_mov_b32 v0, v0\n"
    "s_nop 0\n"
    "v_mov_b32 v0, v0\n"
    "s_nop 1\n"
    "v_mov_b32 v0, v0\n"
    "s_nop 0\n"
    "v_mov_b32 v0, v0\n"
    "s_nop 1\n"
    "v_mov_b32 v0, v0\n"
    "s_nop 0\n"
    "v_mov_b32 v0, v0\n"
    "s_nop 2\n"
    "v_mov_b32 v0, v0\n"
    "s_nop 0\n"
    "v_mov_b32 v0, v0\n"
    "s_nop 1\n"
    "v_mov_b32 v0, v0\n"
    "s_nop 0\n"
    "v_mov_b32 v0, v0\n"
    "s_nop 1\n"
    "v_mov_b32 v0, v0\n"
    "s_nop 0\n"
    "v_mov_b32 v0, v0\n"
    "s_nop 2\n"
    "v_mov_b32 v0, v0\n"
    "s_nop 0\n"
    "v_mov_b32 v0, v0\n"
    "s_nop 1\n"
    "v_mov_b32 v0, v0\n"
    "s_nop 0\n"
    "v_mov_b32 v0, v0\n"
    "s_nop 1\n"
    "v_mov_b32 v0, v0\n"
    "s_nop 0\n"
    "v_mov_b32 v0, v0\n"
    "s_nop 2\n"
    "v_mov_b32 v0, v0\n"
    "s_nop 0\n"
    "v_mov_b32 v0, v0\n"
    "tail1_%=:\n"
    "s_cbranch_execz dryret_%=\n"
    "ds_read_b128 v[88:91], v0 offset:1024\n"
    "buffer_load_dwordx4 v[108:111], v0, s[8:11], s50 offen sc1\n"
    "buffer_load_dwordx4 v[104:107], v0, s[8:11], s50 offen offset:1024 sc1\n"
    "v_swap_b32 v57, v58\n"
    "s_waitcnt lgkmcnt(3)\n"
    "v_mfma_f32_16x16x32_bf16 v[84:87], v[68:71], v[100:103], 0\n"
    "ds_read_b128 v[80:83], v0 offset:9216\n"
    "v_swap_b32 v58, v60\n"
    "s_waitcnt lgkmcnt(3)\n"
    "v_mfma_f32_16x16x32_bf16 v[72:75], v[28:31], v[100:103], 0\n"
    "ds_read_b128 v[68:71], v0 offset:5120\n"
    "v_swap_b32 v59, v62\n"
    "s_waitcnt lgkmcnt(3)\n"
    "v_mfma_f32_16x16x32_bf16 v[64:67], v[96:99], v[20:23], 0\n"
    "ds_read_b128 v[28:31], v0 offset:13376\n"
    "v_swap_b32 v61, v62\n"
    "v_mfma_f32_16x16x32_bf16 v[24:27], v[96:99], v[100:103], 0\n"
    "ds_read_b128 v[20:23], v117 offset:640\n"
    "s_waitcnt lgkmcnt(3)\n"
    "v_mfma_f32_16x16x32_bf16 v[92:95], v[80:83], v[88:91], v[92:95]\n"
    "ds_read_b128 v[16:19], v0 offset:2048\n"
    "v_swap_b32 v49, v50\n"
    "s_waitcnt lgkmcnt(3)\n"
    "v_mfma_f32_16x16x32_bf16 v[84:87], v[80:83], v[68:71], v[84:87]\n"
    "ds_read_b128 v[100:103], v0 offset:10240\n"
    "v_swap_b32 v50, v52\n"
    "s_waitcnt lgkmcnt(3)\n"
    "v_mfma_f32_16x16x32_bf16 v[72:75], v[28:31], v[68:71], v[72:75]\n"
    "ds_read_b128 v[96:99], v0 offset:6144\n"
    "v_swap_b32 v51, v54\n"
    "s_waitcnt lgkmcnt(3)\n"
    "v_mfma_f32_16x16x32_bf16 v[64:67], v[20:23], v[88:91], v[64:67]\n"
    "ds_read_b128 v[80:83], v0 offset:14400\n"
    "v_swap_b32 v53, v54\n"
    "v_mfma_f32_16x16x32_bf16 v[24:27], v[20:23], v[68:71], v[24:27]\n"
    "ds_read_b128 v[88:91], v117 offset:704\n"
    "s_waitcnt lgkmcnt(3)\n"
    "v_mfma_f32_16x16x32_bf16 v[92:95], v[100:103], v[16:19], v[92:95]\n"
    "ds_read_b128 v[28:31], v0 offset:3072\n"
    "v_swap_b32 v41, v42\n"
    "s_waitcnt lgkmcnt(3)\n"
    "v_mfma_f32_16x16x32_bf16 v[84:87], v[100:103], v[96:99], v[84:87]\n"
    "ds_read_b128 v[68:71], v0 offset:11264\n"
    "v_swap_b32 v42, v44\n"
    "s_waitcnt lgkmcnt(3)\n"
    "v_mfma_f32_16x16x32_bf16 v[72:75], v[80:83], v[96:99], v[72:75]\n"
    "ds_read_b128 v[100:103], v0 offset:7168\n"
    "v_swap_b32 v43, v46\n"
    "rgo1_%=:\n"
    "s_waitcnt vmcnt(1)\n"
    "v_mfma_f32_16x16x32_bf16 v[108:111], v[120:123], v[108:111], 0\n"
    "s_waitcnt vmcnt(0)\n"
    "v_mfma_f32_16x16x32_bf16 v[108:111], v[120:123], v[104:107], v[108:111]\n"
    "s_nop 3\n"
    "v_mov_b32 v0, v0\n"
    "s_nop 2\n"
    "v_add_f32_dpp v108, v108, v108 row_ror:8 row_mask:0xf bank_mask:0xf\n"
    "v_add_f32_dpp v109, v109, v109 row_ror:8 row_mask:0xf bank_mask:0xf\n"
    "v_cmp_u_f32 vcc, v108, v109\n"
    "v_add_f32_dpp v108, v108, v108 row_ror:4 row_mask:0xf bank_mask:0xf\n"
    "v_add_f32_dpp v109, v109, v109 row_ror:4 row_mask:0xf bank_mask:0xf\n"
    "v_cvt_pk_bf16_f32 v110, v108, v109\n"
    "s_cbranch_vccnz slowR1_%=\n"
    "rok1_%=:\n"
    "buffer_store_dword v110, v78, s[8:11], s47 offen sc1\n"
    "s_waitcnt lgkmcnt(3)\n"
    "v_mfma_f32_16x16x32_bf16 v[64:67], v[88:91], v[16:19], v[64:67]\n"
    "ds_read_b128 v[80:83], v0 offset:15424\n"
    "v_swap_b32 v45, v46\n"
    "v_mfma_f32_16x16x32_bf16 v[24:27], v[88:91], v[96:99], v[24:27]\n"
    "ds_read_b128 v[20:23], v117 offset:768\n"
    "s_waitcnt lgkmcnt(3)\n"
    "v_mfma_f32_16x16x32_bf16 v[92:95], v[68:71], v[28:31], v[92:95]\n"
    "s_waitcnt lgkmcnt(2)\n"
    "v_swap_b32 v33, v34\n"
    "v_mfma_f32_16x16x32_bf16 v[84:87], v[68:71], v[100:103], v[84:87]\n"
    "s_waitcnt lgkmcnt(1)\n"
    "v_swap_b32 v34, v36\n"
    "v_mfma_f32_16x16x32_bf16 v[72:75], v[80:83], v[100:103], v[72:75]\n"
    "s_waitcnt lgkmcnt(0)\n"
    "v_swap_b32 v35, v38\n"
    "v_mfma_f32_16x16x32_bf16 v[64:67], v[20:23], v[28:31], v[64:67]\n"
    "s_waitcnt lgkmcnt(0)\n"
    "s_barrier\n"
    "s_add_u32 s45, s45, 0x10000\n"
    "v_swap_b32 v37, v38\n"
    "s_lshl_b32 m0, s67, 1\n"
    "s_add_u32 s62, s45, 0\n"
    "v_mfma_f32_16x16x32_bf16 v[24:27], v[20:23], v[100:103], v[24:27]\n"
    "buffer_load_dwordx4 v0, s[8:11], s62 offen sc1 lds\n"
    "buffer_load_dwordx4 v0, s[8:11], s62 offen offset:1024 sc1 lds\n"
    "v_mov_b32 v2, 0xff800000\n"
    "v_cndmask_b32 v92, v2, v92, s[72:73]\n"
    "v_cndmask_b32 v93, v2, v93, s[74:75]\n"
    "v_cndmask_b32 v94, v2, v94, s[76:77]\n"
    "v_cndmask_b32 v95, v2, v95, s[78:79]\n"
    "v_cndmask_b32 v72, v2, v72, s[72:73]\n"
    "v_cndmask_b32 v73, v2, v73, s[74:75]\n"
    "v_cndmask_b32 v74, v2, v74, s[76:77]\n"
    "v_cndmask_b32 v75, v2, v75, s[78:79]\n"
    "v_max_f32 v4, v92, v93\n"
    "v_max3_f32 v4, v4, v94, v95\n"
    "v_max3_f32 v4, v4, v64, v65\n"
    "v_max_f32 v5, v84, v85\n"
    "v_max3_f32 v5, v5, v86, v87\n"
    "v_max3_f32 v5, v5, v72, v73\n"
    "v_max3_f32 v5, v5, v74, v75\n"
    "v_max3_f32 v5, v5, v24, v25\n"
    "v_and_b32 v2, 0xf0, v0\n"
    "v_lshrrev_b32 v8, 4, v0\n"
    "v_permlane32_swap_b32 v4, v5\n"
    "v_max_f32 v4, v4, v5\n"
    "v_mov_b32 v5, v4\n"
    "v_and_b32 v8, 48, v8\n"
    "v_xor_b32 v8, v8, v2\n"
    "v_permlane16_swap_b32 v4, v5\n"
    "v_max_f32 v4, v4, v5\n"
    "v_mul_f32 v5, s32, v4\n"
    "v_mul_f32 v4, s32, v4\n"
    "v_lshl_or_b32 v2, v2, 4, v8\n"
    "v_lshrrev_b32 v6, 2, v0\n"
    "v_permlane32_swap_b32 v4, v5\n"
    "v_pk_fma_f32 v[92:93], v[92:93], s[32:33], v[4:5] op_sel:[0,0,0] op_sel_hi:[1,0,0] neg_lo:[0,0,1] neg_hi:[0,0,1]\n"
    "v_pk_fma_f32 v[94:95], v[94:95], s[32:33], v[4:5] op_sel:[0,0,0] op_sel_hi:[1,0,0] neg_lo:[0,0,1] neg_hi:[0,0,1]\n"
    "v_pk_fma_f32 v[64:65], v[64:65], s[32:33], v[4:5] op_sel:[0,0,0] op_sel_hi:[1,0,0] neg_lo:[0,0,1] neg_hi:[0,0,1]\n"
    "v_pk_fma_f32 v[84:85], v[84:85], s[32:33], v[4:5] op_sel:[0,0,1] op_sel_hi:[1,0,1] neg_lo:[0,0,1] neg_hi:[0,0,1]\n"
    "v_pk_fma_f32 v[86:87], v[86:87], s[32:33], v[4:5] op_sel:[0,0,1] op_sel_hi:[1,0,1] neg_lo:[0,0,1] neg_hi:[0,0,1]\n"
    "v_pk_fma_f32 v[72:73], v[72:73], s[32:33], v[4:5] op_sel:[0,0,1] op_sel_hi:[1,0,1] neg_lo:[0,0,1] neg_hi:[0,0,1]\n"
    "v_pk_fma_f32 v[74:75], v[74:75], s[32:33], v[4:5] op_sel:[0,0,1] op_sel_hi:[1,0,1] neg_lo:[0,0,1] neg_hi:[0,0,1]\n"
    "v_pk_fma_f32 v[24:25], v[24:25], s[32:33], v[4:5] op_sel:[0,0,1] op_sel_hi:[1,0,1] neg_lo:[0,0,1] neg_hi:[0,0,1]\n"
    "v_exp_f32 v92, v92\n"
    "v_exp_f32 v93, v93\n"
    "v_exp_f32 v94, v94\n"
    "v_exp_f32 v95, v95\n"
    "v_exp_f32 v64, v64\n"
    "v_exp_f32 v65, v65\n"
    "v_exp_f32 v84, v84\n"
    "v_exp_f32 v85, v85\n"
    "v_exp_f32 v86, v86\n"
    "v_exp_f32 v87, v87\n"
    "v_exp_f32 v72, v72\n"
    "v_exp_f32 v73, v73\n"
    "v_exp_f32 v74, v74\n"
    "v_exp_f32 v75, v75\n"
    "v_exp_f32 v24, v24\n"
    "v_exp_f32 v25, v25\n"
    "v_pk_add_f32 v[66:67], v[92:93], v[94:95]\n"
    "v_pk_add_f32 v[66:67], v[66:67], v[64:65]\n"
    "v_add_f32 v4, v66, v67\n"
    "v_pk_add_f32 v[26:27], v[84:85], v[86:87]\n"
    "v_pk_add_f32 v[26:27], v[26:27], v[72:73]\n"
    "v_pk_add_f32 v[26:27], v[26:27], v[74:75]\n"
    "v_pk_add_f32 v[26:27], v[26:27], v[24:25]\n"
    "v_add_f32 v5, v26, v27\n"
    "v_mov_b64 v[68:69], -1\n"
    "v_mov_b64 v[70:71], -1\n"
    "v_permlane32_swap_b32 v4, v5\n"
    "v_add_f32 v4, v4, v5\n"
    "v_mov_b32 v5, v4\n"
    "v_mov_b32 v117, 0\n"
    "v_mov_b32 v118, 0\n"
    "v_permlane16_swap_b32 v4, v5\n"
    "v_add_f32 v4, v4, v5\n"
    "v_rcp_f32 v4, v4\n"
    "s_nop 0\n"
    "v_mov_b32 v5, v4\n"
    "v_mov_b32 v103, 0\n"
    "s_nop 0\n"
    "v_permlane32_swap_b32 v4, v5\n"
    "v_pk_mul_f32 v[92:93], v[92:93], v[4:5] op_sel:[0,0] op_sel_hi:[1,0]\n"
    "v_pk_mul_f32 v[94:95], v[94:95], v[4:5] op_sel:[0,0] op_sel_hi:[1,0]\n"
    "v_pk_mul_f32 v[64:65], v[64:65], v[4:5] op_sel:[0,0] op_sel_hi:[1,0]\n"
    "v_pk_mul_f32 v[84:85], v[84:85], v[4:5] op_sel:[0,1] op_sel_hi:[1,1]\n"
    "v_pk_mul_f32 v[86:87], v[86:87], v[4:5] op_sel:[0,1] op_sel_hi:[1,1]\n"
    "v_pk_mul_f32 v[72:73], v[72:73], v[4:5] op_sel:[0,1] op_sel_hi:[1,1]\n"
    "v_pk_mul_f32 v[74:75], v[74:75], v[4:5] op_sel:[0,1] op_sel_hi:[1,1]\n"
    "v_pk_mul_f32 v[24:25], v[24:25], v[4:5] op_sel:[0,1] op_sel_hi:[1,1]\n"
    "s_mov_b32 s14, 0x3400000\n"
    "v_cvt_pk_bf16_f32 v28, v84, v85\n"
    "v_cvt_pk_bf16_f32 v29, v86, v87\n"
    "v_cvt_pk_bf16_f32 v30, v72, v73\n"
    "v_cvt_pk_bf16_f32 v31, v74, v75\n"
    "s_add_u32 m0, m0, 0x2000\n"
    "s_add_u32 s62, s45, 0x4000\n"
    "v_mfma_f32_16x16x32_bf16 v[120:123], v[28:31], v[56:59], 0\n"
    "buffer_load_dwordx4 v0, s[8:11], s62 offen sc1 lds\n"
    "v_mfma_f32_16x16x32_bf16 v[108:111], v[28:31], v[60:63], 0\n"
    "v_cvt_pk_bf16_f32 v116, v24, v25\n"
    "v_cvt_pk_bf16_f32 v119, v24, v25\n"
    "v_mfma_f32_16x16x32_bf16 v[104:107], v[28:31], v[48:51], 0\n"
    "buffer_store_dwordx4 v[68:71], v0, s[8:11], s50 offen sc1\n"
    "v_mfma_f32_16x16x32_bf16 v[96:99], v[28:31], v[52:55], 0\n"
    "v_cvt_pk_bf16_f32 v100, v92, v93\n"
    "v_cvt_pk_bf16_f32 v101, v94, v95\n"
    "v_mfma_f32_16x16x32_bf16 v[88:91], v[28:31], v[40:43], 0\n"
    "buffer_store_dwordx4 v[68:71], v0, s[8:11], s50 offen offset:1024 sc1\n"
    "s_add_u32 s61, s46, s49\n"
    "v_mfma_f32_16x16x32_bf16 v[92:95], v[28:31], v[44:47], 0\n"
    "v_cvt_pk_bf16_f32 v102, v64, v65\n"
    "v_mov_b32 v58, v124\n"
    "v_mfma_f32_16x16x32_bf16 v[84:87], v[28:31], v[32:35], 0\n"
    "buffer_store_dword v68, v6, s[8:11], s61 offen sc1\n"
    "v_mfma_f32_16x16x32_bf16 v[80:83], v[28:31], v[36:39], 0\n"
    "buffer_load_dwordx4 v0, s[8:11], s62 offen offset:1024 sc1 lds\n"
    "v_mfma_f32_16x16x16_bf16 v[120:123], v[116:117], v[124:125], v[120:123]\n"
    "v_mov_b32 v62, v125\n"
    "v_mov_b32 v50, v126\n"
    "v_mov_b32 v54, v127\n"
    "v_mfma_f32_16x16x16_bf16 v[108:111], v[118:119], v[124:125], v[108:111]\n"
    "v_mov_b32 v42, v112\n"
    "v_mov_b32 v46, v113\n"
    "v_mov_b32 v34, v114\n"
    "v_mfma_f32_16x16x16_bf16 v[104:107], v[116:117], v[126:127], v[104:107]\n"
    "v_mov_b32 v38, v115\n"
    "v_mfma_f32_16x16x16_bf16 v[96:99], v[118:119], v[126:127], v[96:99]\n"
    "v_cvt_pk_bf16_f32 v72, v120, v108\n"
    "v_cvt_pk_bf16_f32 v68, v121, v109\n"
    "v_cvt_pk_bf16_f32 v64, v122, v110\n"
    "v_mfma_f32_16x16x16_bf16 v[88:91], v[116:117], v[112:113], v[88:91]\n"
    "v_cvt_pk_bf16_f32 v28, v123, v111\n"
    "v_mfma_f32_16x16x16_bf16 v[92:95], v[118:119], v[112:113], v[92:95]\n"
    "v_cvt_pk_bf16_f32 v73, v104, v96\n"
    "v_cvt_pk_bf16_f32 v69, v105, v97\n"
    "v_cvt_pk_bf16_f32 v65, v106, v98\n"
    "v_mfma_f32_16x16x16_bf16 v[84:87], v[116:117], v[114:115], v[84:87]\n"
    "v_cvt_pk_bf16_f32 v29, v107, v99\n"
    "v_mfma_f32_16x16x16_bf16 v[80:83], v[118:119], v[114:115], v[80:83]\n"
    "v_cvt_pk_bf16_f32 v74, v88, v92\n"
    "v_cvt_pk_bf16_f32 v70, v89, v93\n"
    "v_cvt_pk_bf16_f32 v66, v90, v94\n"
    "v_mfma_f32_16x16x32_bf16 v[124:127], v[100:103], v[56:59], 0\n"
    "s_waitcnt vmcnt(5)\n"
    "s_branch join_%=\n"
    "s_nop 1\n"
    "v_mov_b32 v0, v0\n"
    "s_nop 0\n"
    "v_mov_b32 v0, v0\n"
    "s_nop 1\n"
    "v_mov_b32 v0, v0\n"
    "s_nop 0\n"
    "v_mov_b32 v0, v0\n"
    "s_nop 2\n"
    "v_mov_b32 v0, v0\n"
    "s_nop 0\n"
    "v_mov_b32 v0, v0\n"
    "s_nop 1\n"
    "v_mov_b32 v0, v0\n"
    "s_nop 0\n"
    "v_mov_b32 v0, v0\n"
    "s_nop 1\n"
    "v_mov_b32 v0, v0\n"
    "s_nop 0\n"
    "v_mov_b32 v0, v0\n"
    "s_nop 2\n"
    "v_mov_b32 v0, v0\n"
    "s_nop 0\n"
    "v_mov_b32 v0, v0\n"
    "s_nop 1\n"
    "v_mov_b32 v0, v0\n"
    "s_nop 0\n"
    "v_mov_b32 v0, v0\n"
    "s_nop 1\n"
    "v_mov_b32 v0, v0\n"
    "s_nop 0\n"
    "v_mov_b32 v0, v0\n"
    "s_nop 2\n"
    "v_mov_b32 v0, v0\n"
    "s_nop 0\n"
    "v_mov_b32 v0, v0\n"
    "s_nop 1\n"
    "v_mov_b32 v0, v0\n"
    "s_nop 0\n"
    "v_mov_b32 v0, v0\n"
    "s_nop 1\n"
    "v_mov_b32 v0, v0\n"
    "s_nop 0\n"
    "v_mov_b32 v0, v0\n"
    "s_nop 2\n"
    "v_mov_b32 v0, v0\n"
    "s_nop 0\n"
    "v_mov_b32 v0, v0\n"
    "s_nop 1\n"
    "v_mov_b32 v0, v0\n"
    "s_nop 0\n"
    "v_mov_b32 v0, v0\n"
    "s_nop 1\n"
    "v_mov_b32 v0, v0\n"
    "s_nop 0\n"
    "v_mov_b32 v0, v0\n"
    "s_nop 2\n"
    "v_mov_b32 v0, v0\n"
    "s_nop 0\n"
    "v_mov_b32 v0, v0\n"
    "s_nop 1\n"
    "v_mov_b32 v0, v0\n"
    "s_nop 0\n"
    "v_mov_b32 v0, v0\n"
    "s_nop 1\n"
    "v_mov_b32 v0, v0\n"
    "s_nop 0\n"
    "v_mov_b32 v0, v0\n"
    "s_nop 2\n"
    "v_mov_b32 v0, v0\n"
    "s_nop 0\n"
    "v_mov_b32 v0, v0\n"
    "s_nop 1\n"
    "v_mov_b32 v0, v0\n"
    "s_nop 0\n"
    "v_mov_b32 v0, v0\n"
    "s_nop 1\n"
    "v_mov_b32 v0, v0\n"
    "s_nop 0\n"
    "v_mov_b32 v0, v0\n"
    "s_nop 2\n"
    "v_mov_b32 v0, v0\n"
    "s_nop 0\n"
    "v_mov_b32 v0, v0\n"
    "s_nop 1\n"
    "v_mov_b32 v0, v0\n"
    "s_nop 0\n"
    "v_mov_b32 v0, v0\n"
    "s_nop 1\n"
    "v_mov_b32 v0, v0\n"
    "s_nop 0\n"
    "v_mov_b32 v0, v0\n"
    "s_nop 2\n"
    "v_mov_b32 v0, v0\n"
    "s_nop 0\n"
    "v_mov_b32 v0, v0\n"
    "tail2_%=:\n"
    "s_cbranch_execz dryret_%=\n"
    "ds_read_b128 v[88:91], v0 offset:1024\n"
    "v_swap_b32 v57, v58\n"
    "s_waitcnt lgkmcnt(3)\n"
    "v_mfma_f32_16x16x32_bf16 v[84:87], v[68:71], v[100:103], 0\n"
    "ds_read_b128 v[80:83], v0 offset:9216\n"
    "v_swap_b32 v58, v60\n"
    "s_waitcnt lgkmcnt(3)\n"
    "v_mfma_f32_16x16x32_bf16 v[72:75], v[28:31], v[100:103], 0\n"
    "ds_read_b128 v[68:71], v0 offset:5120\n"
    "v_swap_b32 v59, v62\n"
    "s_waitcnt lgkmcnt(3)\n"
    "v_mfma_f32_16x16x32_bf16 v[64:67], v[96:99], v[20:23], 0\n"
    "ds_read_b128 v[28:31], v0 offset:13376\n"
    "v_swap_b32 v61, v62\n"
    "v_mfma_f32_16x16x32_bf16 v[24:27], v[96:99], v[100:103], 0\n"
    "ds_read_b128 v[20:23], v117 offset:640\n"
    "s_waitcnt lgkmcnt(3)\n"
    "v_mfma_f32_16x16x32_bf16 v[92:95], v[80:83], v[88:91], v[92:95]\n"
    "ds_read_b128 v[16:19], v0 offset:2048\n"
    "v_swap_b32 v49, v50\n"
    "buffer_load_dwordx4 v[108:111], v0, s[8:11], s50 offen sc1\n"
    "buffer_load_dwordx4 v[104:107], v0, s[8:11], s50 offen offset:1024 sc1\n"
    "s_waitcnt lgkmcnt(3)\n"
    "v_mfma_f32_16x16x32_bf16 v[84:87], v[80:83], v[68:71], v[84:87]\n"
    "ds_read_b128 v[100:103], v0 offset:10240\n"
    "v_swap_b32 v50, v52\n"
    "s_waitcnt lgkmcnt(3)\n"
    "v_mfma_f32_16x16x32_bf16 v[72:75], v[28:31], v[68:71], v[72:75]\n"
    "ds_read_b128 v[96:99], v0 offset:6144\n"
    "v_swap_b32 v51, v54\n"
    "s_waitcnt lgkmcnt(3)\n"
    "v_mfma_f32_16x16x32_bf16 v[64:67], v[20:23], v[88:91], v[64:67]\n"
    "ds_read_b128 v[80:83], v0 offset:14400\n"
    "v_swap_b32 v53, v54\n"
    "v_mfma_f32_16x16x32_bf16 v[24:27], v[20:23], v[68:71], v[24:27]\n"
    "ds_read_b128 v[88:91], v117 offset:704\n"
    "s_waitcnt lgkmcnt(3)\n"
    "v_mfma_f32_16x16x32_bf16 v[92:95], v[100:103], v[16:19], v[92:95]\n"
    "ds_read_b128 v[28:31], v0 offset:3072\n"
    "v_swap_b32 v41, v42\n"
    "s_waitcnt lgkmcnt(3)\n"
    "v_mfma_f32_16x16x32_bf16 v[84:87], v[100:103], v[96:99], v[84:87]\n"
    "ds_read_b128 v[68:71], v0 offset:11264\n"
    "v_swap_b32 v42, v44\n"
    "s_waitcnt lgkmcnt(3)\n"
    "v_mfma_f32_16x16x32_bf16 v[72:75], v[80:83], v[96:99], v[72:75]\n"
    "ds_read_b128 v[100:103], v0 offset:7168\n"
    "v_swap_b32 v43, v46\n"
    "s_waitcnt lgkmcnt(3)\n"
    "v_mfma_f32_16x16x32_bf16 v[64:67], v[88:91], v[16:19], v[64:67]\n"
    "ds_read_b128 v[80:83], v0 offset:15424\n"
    "v_swap_b32 v45, v46\n"
    "v_mfma_f32_16x16x32_bf16 v[24:27], v[88:91], v[96:99], v[24:27]\n"
    "ds_read_b128 v[20:23], v117 offset:768\n"
    "s_waitcnt lgkmcnt(3)\n"
    "v_mfma_f32_16x16x32_bf16 v[92:95], v[68:71], v[28:31], v[92:95]\n"
    "s_waitcnt lgkmcnt(2)\n"
    "v_swap_b32 v33, v34\n"
    "v_mfma_f32_16x16x32_bf16 v[84:87], v[68:71], v[100:103], v[84:87]\n"
    "s_waitcnt lgkmcnt(1)\n"
    "v_swap_b32 v34, v36\n"
    "v_mfma_f32_16x16x32_bf16 v[72:75], v[80:83], v[100:103], v[72:75]\n"
    "s_waitcnt lgkmcnt(0)\n"
    "v_swap_b32 v35, v38\n"
    "v_mfma_f32_16x16x32_bf16 v[64:67], v[20:23], v[28:31], v[64:67]\n"
    "s_waitcnt lgkmcnt(0)\n"
    "s_barrier\n"
    "s_add_u32 s45, s45, 0x10000\n"
    "v_swap_b32 v37, v38\n"
    "s_lshl_b32 m0, s67, 1\n"
    "s_add_u32 s62, s45, 0\n"
    "v_mfma_f32_16x16x32_bf16 v[24:27], v[20:23], v[100:103], v[24:27]\n"
    "rgo2_%=:\n"
    "s_waitcnt vmcnt(1)\n"
    "v_mfma_f32_16x16x32_bf16 v[108:111], v[120:123], v[108:111], 0\n"
    "s_waitcnt vmcnt(0)\n"
    "v_mfma_f32_16x16x32_bf16 v[108:111], v[120:123], v[104:107], v[108:111]\n"
    "s_nop 3\n"
    "v_mov_b32 v0, v0\n"
    "s_nop 2\n"
    "v_add_f32_dpp v108, v108, v108 row_ror:8 row_mask:0xf bank_mask:0xf\n"
    "v_add_f32_dpp v109, v109, v109 row_ror:8 row_mask:0xf bank_mask:0xf\n"
    "v_cmp_u_f32 vcc, v108, v109\n"
    "v_add_f32_dpp v108, v108, v108 row_ror:4 row_mask:0xf bank_mask:0xf\n"
    "v_add_f32_dpp v109, v109, v109 row_ror:4 row_mask:0xf bank_mask:0xf\n"
    "v_cvt_pk_bf16_f32 v110, v108, v109\n"
    "s_cbranch_vccnz slowR2_%=\n"
    "rok2_%=:\n"
    "buffer_store_dword v110, v78, s[8:11], s47 offen sc1\n"
    "buffer_load_dwordx4 v0, s[8:11], s62 offen sc1 lds\n"
    "buffer_load_dwordx4 v0, s[8:11], s62 offen offset:1024 sc1 lds\n"
    "v_mov_b32 v2, 0xff800000\n"
    "v_cndmask_b32 v92, v2, v92, s[72:73]\n"
    "v_cndmask_b32 v93, v2, v93, s[74:75]\n"
    "v_cndmask_b32 v94, v2, v94, s[76:77]\n"
    "v_cndmask_b32 v95, v2, v95, s[78:79]\n"
    "v_cndmask_b32 v72, v2, v72, s[72:73]\n"
    "v_cndmask_b32 v73, v2, v73, s[74:75]\n"
    "v_cndmask_b32 v74, v2, v74, s[76:77]\n"
    "v_cndmask_b32 v75, v2, v75, s[78:79]\n"
    "v_max_f32 v4, v92, v93\n"
    "v_max3_f32 v4, v4, v94, v95\n"
    "v_max3_f32 v4, v4, v64, v65\n"
    "v_max_f32 v5, v84, v85\n"
    "v_max3_f32 v5, v5, v86, v87\n"
    "v_max3_f32 v5, v5, v72, v73\n"
    "v_max3_f32 v5, v5, v74, v75\n"
    "v_max3_f32 v5, v5, v24, v25\n"
    "v_and_b32 v2, 0xf0, v0\n"
    "v_lshrrev_b32 v8, 4, v0\n"
    "v_permlane32_swap_b32 v4, v5\n"
    "v_max_f32 v4, v4, v5\n"
    "v_mov_b32 v5, v4\n"
    "v_and_b32 v8, 48, v8\n"
    "v_xor_b32 v8, v8, v2\n"
    "v_permlane16_swap_b32 v4, v5\n"
    "v_max_f32 v4, v4, v5\n"
    "v_mul_f32 v5, s32, v4\n"
    "v_mul_f32 v4, s32, v4\n"
    "v_lshl_or_b32 v2, v2, 4, v8\n"
    "v_lshrrev_b32 v6, 2, v0\n"
    "v_permlane32_swap_b32 v4, v5\n"
    "v_pk_fma_f32 v[92:93], v[92:93], s[32:33], v[4:5] op_sel:[0,0,0] op_sel_hi:[1,0,0] neg_lo:[0,0,1] neg_hi:[0,0,1]\n"
    "v_pk_fma_f32 v[94:95], v[94:95], s[32:33], v[4:5] op_sel:[0,0,0] op_sel_hi:[1,0,0] neg_lo:[0,0,1] neg_hi:[0,0,1]\n"
    "v_pk_fma_f32 v[64:65], v[64:65], s[32:33], v[4:5] op_sel:[0,0,0] op_sel_hi:[1,0,0] neg_lo:[0,0,1] neg_hi:[0,0,1]\n"
    "v_pk_fma_f32 v[84:85], v[84:85], s[32:33], v[4:5] op_sel:[0,0,1] op_sel_hi:[1,0,1] neg_lo:[0,0,1] neg_hi:[0,0,1]\n"
    "v_pk_fma_f32 v[86:87], v[86:87], s[32:33], v[4:5] op_sel:[0,0,1] op_sel_hi:[1,0,1] neg_lo:[0,0,1] neg_hi:[0,0,1]\n"
    "v_pk_fma_f32 v[72:73], v[72:73], s[32:33], v[4:5] op_sel:[0,0,1] op_sel_hi:[1,0,1] neg_lo:[0,0,1] neg_hi:[0,0,1]\n"
    "v_pk_fma_f32 v[74:75], v[74:75], s[32:33], v[4:5] op_sel:[0,0,1] op_sel_hi:[1,0,1] neg_lo:[0,0,1] neg_hi:[0,0,1]\n"
    "v_pk_fma_f32 v[24:25], v[24:25], s[32:33], v[4:5] op_sel:[0,0,1] op_sel_hi:[1,0,1] neg_lo:[0,0,1] neg_hi:[0,0,1]\n"
    "v_exp_f32 v92, v92\n"
    "v_exp_f32 v93, v93\n"
    "v_exp_f32 v94, v94\n"
    "v_exp_f32 v95, v95\n"
    "v_exp_f32 v64, v64\n"
    "v_exp_f32 v65, v65\n"
    "v_exp_f32 v84, v84\n"
    "v_exp_f32 v85, v85\n"
    "v_exp_f32 v86, v86\n"
    "v_exp_f32 v87, v87\n"
    "v_exp_f32 v72, v72\n"
    "v_exp_f32 v73, v73\n"
    "v_exp_f32 v74, v74\n"
    "v_exp_f32 v75, v75\n"
    "v_exp_f32 v24, v24\n"
    "v_exp_f32 v25, v25\n"
    "v_pk_add_f32 v[66:67], v[92:93], v[94:95]\n"
    "v_pk_add_f32 v[66:67], v[66:67], v[64:65]\n"
    "v_add_f32 v4, v66, v67\n"
    "v_pk_add_f32 v[26:27], v[84:85], v[86:87]\n"
    "v_pk_add_f32 v[26:27], v[26:27], v[72:73]\n"
    "v_pk_add_f32 v[26:27], v[26:27], v[74:75]\n"
    "v_pk_add_f32 v[26:27], v[26:27], v[24:25]\n"
    "v_add_f32 v5, v26, v27\n"
    "v_mov_b64 v[68:69], -1\n"
    "v_mov_b64 v[70:71], -1\n"
    "v_permlane32_swap_b32 v4, v5\n"
    "v_add_f32 v4, v4, v5\n"
    "v_mov_b32 v5, v4\n"
    "v_mov_b32 v117, 0\n"
    "v_mov_b32 v118, 0\n"
    "v_permlane16_swap_b32 v4, v5\n"
    "v_add_f32 v4, v4, v5\n"
    "v_rcp_f32 v4, v4\n"
    "s_nop 0\n"
    "v_mov_b32 v5, v4\n"
    "v_mov_b32 v103, 0\n"
    "s_nop 0\n"
    "v_permlane32_swap_b32 v4, v5\n"
    "v_pk_mul_f32 v[92:93], v[92:93], v[4:5] op_sel:[0,0] op_sel_hi:[1,0]\n"
    "v_pk_mul_f32 v[94:95], v[94:95], v[4:5] op_sel:[0,0] op_sel_hi:[1,0]\n"
    "v_pk_mul_f32 v[64:65], v[64:65], v[4:5] op_sel:[0,0] op_sel_hi:[1,0]\n"
    "v_pk_mul_f32 v[84:85], v[84:85], v[4:5] op_sel:[0,1] op_sel_hi:[1,1]\n"
    "v_pk_mul_f32 v[86:87], v[86:87], v[4:5] op_sel:[0,1] op_sel_hi:[1,1]\n"
    "v_pk_mul_f32 v[72:73], v[72:73], v[4:5] op_sel:[0,1] op_sel_hi:[1,1]\n"
    "v_pk_mul_f32 v[74:75], v[74:75], v[4:5] op_sel:[0,1] op_sel_hi:[1,1]\n"
    "v_pk_mul_f32 v[24:25], v[24:25], v[4:5] op_sel:[0,1] op_sel_hi:[1,1]\n"
    "s_mov_b32 s14, 0x3400000\n"
    "v_cvt_pk_bf16_f32 v28, v84, v85\n"
    "v_cvt_pk_bf16_f32 v29, v86, v87\n"
    "v_cvt_pk_bf16_f32 v30, v72, v73\n"
    "v_cvt_pk_bf16_f32 v31, v74, v75\n"
    "s_add_u32 m0, m0, 0x2000\n"
    "s_add_u32 s62, s45, 0x4000\n"
    "v_mfma_f32_16x16x32_bf16 v[120:123], v[28:31], v[56:59], 0\n"
    "buffer_load_dwordx4 v0, s[8:11], s62 offen sc1 lds\n"
    "v_mfma_f32_16x16x32_bf16 v[108:111], v[28:31], v[60:63], 0\n"
    "v_cvt_pk_bf16_f32 v116, v24, v25\n"
    "v_cvt_pk_bf16_f32 v119, v24, v25\n"
    "v_mfma_f32_16x16x32_bf16 v[104:107], v[28:31], v[48:51], 0\n"
    "buffer_store_dwordx4 v[68:71], v0, s[8:11], s50 offen sc1\n"
    "v_mfma_f32_16x16x32_bf16 v[96:99], v[28:31], v[52:55], 0\n"
    "v_cvt_pk_bf16_f32 v100, v92, v93\n"
    "v_cvt_pk_bf16_f32 v101, v94, v95\n"
    "v_mfma_f32_16x16x32_bf16 v[88:91], v[28:31], v[40:43], 0\n"
    "buffer_store_dwordx4 v[68:71], v0, s[8:11], s50 offen offset:1024 sc1\n"
    "s_add_u32 s61, s46, s49\n"
    "v_mfma_f32_16x16x32_bf16 v[92:95], v[28:31], v[44:47], 0\n"
    "v_cvt_pk_bf16_f32 v102, v64, v65\n"
    "v_mov_b32 v58, v124\n"
    "v_mfma_f32_16x16x32_bf16 v[84:87], v[28:31], v[32:35], 0\n"
    "buffer_store_dword v68, v6, s[8:11], s61 offen sc1\n"
    "v_mfma_f32_16x16x32_bf16 v[80:83], v[28:31], v[36:39], 0\n"
    "buffer_load_dwordx4 v0, s[8:11], s62 offen offset:1024 sc1 lds\n"
    "v_mfma_f32_16x16x16_bf16 v[120:123], v[116:117], v[124:125], v[120:123]\n"
    "v_mov_b32 v62, v125\n"
    "v_mov_b32 v50, v126\n"
    "v_mov_b32 v54, v127\n"
    "v_mfma_f32_16x16x16_bf16 v[108:111], v[118:119], v[124:125], v[108:111]\n"
    "v_mov_b32 v42, v112\n"
    "v_mov_b32 v46, v113\n"
    "v_mov_b32 v34, v114\n"
    "v_mfma_f32_16x16x16_bf16 v[104:107], v[116:117], v[126:127], v[104:107]\n"
    "v_mov_b32 v38, v115\n"
    "v_mfma_f32_16x16x16_bf16 v[96:99], v[118:119], v[126:127], v[96:99]\n"
    "v_cvt_pk_bf16_f32 v72, v120, v108\n"
    "v_cvt_pk_bf16_f32 v68, v121, v109\n"
    "v_cvt_pk_bf16_f32 v64, v122, v110\n"
    "v_mfma_f32_16x16x16_bf16 v[88:91], v[116:117], v[112:113], v[88:91]\n"
    "v_cvt_pk_bf16_f32 v28, v123, v111\n"
    "v_mfma_f32_16x16x16_bf16 v[92:95], v[118:119], v[112:113], v[92:95]\n"
    "v_cvt_pk_bf16_f32 v73, v104, v96\n"
    "v_cvt_pk_bf16_f32 v69, v105, v97\n"
    "v_cvt_pk_bf16_f32 v65, v106, v98\n"
    "v_mfma_f32_16x16x16_bf16 v[84:87], v[116:117], v[114:115], v[84:87]\n"
    "v_cvt_pk_bf16_f32 v29, v107, v99\n"
    "v_mfma_f32_16x16x16_bf16 v[80:83], v[118:119], v[114:115], v[80:83]\n"
    "v_cvt_pk_bf16_f32 v74, v88, v92\n"
    "v_cvt_pk_bf16_f32 v70, v89, v93\n"
    "v_cvt_pk_bf16_f32 v66, v90, v94\n"
    "v_mfma_f32_16x16x32_bf16 v[124:127], v[100:103], v[56:59], 0\n"
    "s_waitcnt vmcnt(5)\n"
    "s_branch join_%=\n"
    "s_nop 1\n"
    "v_mov_b32 v0, v0\n"
    "s_nop 0\n"
    "v_mov_b32 v0, v0\n"
    "s_nop 1\n"
    "v_mov_b32 v0, v0\n"
    "s_nop 0\n"
    "v_mov_b32 v0, v0\n"
    "s_nop 2\n"
    "v_mov_b32 v0, v0\n"
    "s_nop 0\n"
    "v_mov_b32 v0, v0\n"
    "s_nop 1\n"
    "v_mov_b32 v0, v0\n"
    "s_nop 0\n"
    "v_mov_b32 v0, v0\n"
    "s_nop 1\n"
    "v_mov_b32 v0, v0\n"
    "s_nop 0\n"
    "v_mov_b32 v0, v0\n"
    "s_nop 2\n"
    "v_mov_b32 v0, v0\n"
    "s_nop 0\n"
    "v_mov_b32 v0, v0\n"
    "s_nop 1\n"
    "v_mov_b32 v0, v0\n"
    "s_nop 0\n"
    "v_mov_b32 v0, v0\n"
    "s_nop 1\n"
    "v_mov_b32 v0, v0\n"
    "s_nop 0\n"
    "v_mov_b32 v0, v0\n"
    "s_nop 2\n"
    "v_mov_b32 v0, v0\n"
    "s_nop 0\n"
    "v_mov_b32 v0, v0\n"
    "s_nop 1\n"
    "v_mov_b32 v0, v0\n"
    "s_nop 0\n"
    "v_mov_b32 v0, v0\n"
    "s_nop 1\n"
    "v_mov_b32 v0, v0\n"
    "s_nop 0\n"
    "v_mov_b32 v0, v0\n"
    "s_nop 2\n"
    "v_mov_b32 v0, v0\n"
    "s_nop 0\n"
    "v_mov_b32 v0, v0\n"
    "s_nop 1\n"
    "v_mov_b32 v0, v0\n"
    "s_nop 0\n"
    "v_mov_b32 v0, v0\n"
    "s_nop 1\n"
    "v_mov_b32 v0, v0\n"
    "s_nop 0\n"
    "v_mov_b32 v0, v0\n"
    "s_nop 2\n"
    "v_mov_b32 v0, v0\n"
    "s_nop 0\n"
    "v_mov_b32 v0, v0\n"
    "s_nop 1\n"
    "v_mov_b32 v0, v0\n"
    "s_nop 0\n"
    "v_mov_b32 v0, v0\n"
    "s_nop 1\n"
    "v_mov_b32 v0, v0\n"
    "s_nop 0\n"
    "v_mov_b32 v0, v0\n"
    "s_nop 2\n"
    "v_mov_b32 v0, v0\n"
    "s_nop 0\n"
    "v_mov_b32 v0, v0\n"
    "s_nop 1\n"
    "v_mov_b32 v0, v0\n"
    "s_nop 0\n"
    "v_mov_b32 v0, v0\n"
    "s_nop 1\n"
    "v_mov_b32 v0, v0\n"
    "s_nop 0\n"
    "v_mov_b32 v0, v0\n"
    "s_nop 2\n"
    "v_mov_b32 v0, v0\n"
    "s_nop 0\n"
    "v_mov_b32 v0, v0\n"
    "s_nop 1\n"
    "v_mov_b32 v0, v0\n"
    "s_nop 0\n"
    "v_mov_b32 v0, v0\n"
    "s_nop 1\n"
    "v_mov_b32 v0, v0\n"
    "s_nop 0\n"
    "v_mov_b32 v0, v0\n"
    "s_nop 2\n"
    "v_mov_b32 v0, v0\n"
    "s_nop 0\n"
    "v_mov_b32 v0, v0\n"
    "tail3_%=:\n"
    "s_cbranch_execz dryret_%=\n"
    "ds_read_b128 v[88:91], v0 offset:1024\n"
    "v_swap_b32 v57, v58\n"
    "s_waitcnt lgkmcnt(3)\n"
    "v_mfma_f32_16x16x32_bf16 v[84:87], v[68:71], v[100:103], 0\n"
    "ds_read_b128 v[80:83], v0 offset:9216\n"
    "v_swap_b32 v58, v60\n"
    "s_waitcnt lgkmcnt(3)\n"
    "v_mfma_f32_16x16x32_bf16 v[72:75], v[28:31], v[100:103], 0\n"
    "ds_read_b128 v[68:71], v0 offset:5120\n"
    "v_swap_b32 v59, v62\n"
    "s_waitcnt lgkmcnt(3)\n"
    "v_mfma_f32_16x16x32_bf16 v[64:67], v[96:99], v[20:23], 0\n"
    "ds_read_b128 v[28:31], v0 offset:13376\n"
    "v_swap_b32 v61, v62\n"
    "v_mfma_f32_16x16x32_bf16 v[24:27], v[96:99], v[100:103], 0\n"
    "ds_read_b128 v[20:23], v117 offset:640\n"
    "s_waitcnt lgkmcnt(3)\n"
    "v_mfma_f32_16x16x32_bf16 v[92:95], v[80:83], v[88:91], v[92:95]\n"
    "ds_read_b128 v[16:19], v0 offset:2048\n"
    "v_swap_b32 v49, v50\n"
    "s_waitcnt lgkmcnt(3)\n"
    "v_mfma_f32_16x16x32_bf16 v[84:87], v[80:83], v[68:71], v[84:87]\n"
    "ds_read_b128 v[100:103], v0 offset:10240\n"
    "v_swap_b32 v50, v52\n"
    "s_waitcnt lgkmcnt(3)\n"
    "v_mfma_f32_16x16x32_bf16 v[72:75], v[28:31], v[68:71], v[72:75]\n"
    "ds_read_b128 v[96:99], v0 offset:6144\n"
    "v_swap_b32 v51, v54\n"
    "s_waitcnt lgkmcnt(3)\n"
    "v_mfma_f32_16x16x32_bf16 v[64:67], v[20:23], v[88:91], v[64:67]\n"
    "ds_read_b128 v[80:83], v0 offset:14400\n"
    "v_swap_b32 v53, v54\n"
    "v_mfma_f32_16x16x32_bf16 v[24:27], v[20:23], v[68:71], v[24:27]\n"
    "ds_read_b128 v[88:91], v117 offset:704\n"
    "s_waitcnt lgkmcnt(3)\n"
    "v_mfma_f32_16x16x32_bf16 v[92:95], v[100:103], v[16:19], v[92:95]\n"
    "ds_read_b128 v[28:31], v0 offset:3072\n"
    "v_swap_b32 v41, v42\n"
    "s_waitcnt lgkmcnt(3)\n"
    "v_mfma_f32_16x16x32_bf16 v[84:87], v[100:103], v[96:99], v[84:87]\n"
    "ds_read_b128 v[68:71], v0 offset:11264\n"
    "v_swap_b32 v42, v44\n"
    "s_waitcnt lgkmcnt(3)\n"
    "v_mfma_f32_16x16x32_bf16 v[72:75], v[80:83], v[96:99], v[72:75]\n"
    "ds_read_b128 v[100:103], v0 offset:7168\n"
    "v_swap_b32 v43, v46\n"
    "s_waitcnt lgkmcnt(3)\n"
    "v_mfma_f32_16x16x32_bf16 v[64:67], v[88:91], v[16:19], v[64:67]\n"
    "ds_read_b128 v[80:83], v0 offset:15424\n"
    "v_swap_b32 v45, v46\n"
    "v_mfma_f32_16x16x32_bf16 v[24:27], v[88:91], v[96:99], v[24:27]\n"
    "ds_read_b128 v[20:23], v117 offset:768\n"
    "s_waitcnt lgkmcnt(3)\n"
    "v_mfma_f32_16x16x32_bf16 v[92:95], v[68:71], v[28:31], v[92:95]\n"
    "s_waitcnt lgkmcnt(2)\n"
    "v_swap_b32 v33, v34\n"
    "v_mfma_f32_16x16x32_bf16 v[84:87], v[68:71], v[100:103], v[84:87]\n"
    "v_swap_b32 v34, v36\n"
    "buffer_load_dwordx4 v[108:111], v0, s[8:11], s50 offen sc1\n"
    "buffer_load_dwordx4 v[104:107], v0, s[8:11], s50 offen offset:1024 sc1\n"
    "s_waitcnt lgkmcnt(1)\n"
    "v_mfma_f32_16x16x32_bf16 v[72:75], v[80:83], v[100:103], v[72:75]\n"
    "s_waitcnt lgkmcnt(0)\n"
    "v_swap_b32 v35, v38\n"
    "v_mfma_f32_16x16x32_bf16 v[64:67], v[20:23], v[28:31], v[64:67]\n"
    "s_waitcnt lgkmcnt(0)\n"
    "s_barrier\n"
    "s_add_u32 s45, s45, 0x10000\n"
    "v_swap_b32 v37, v38\n"
    "s_lshl_b32 m0, s67, 1\n"
    "s_add_u32 s62, s45, 0\n"
    "v_mfma_f32_16x16x32_bf16 v[24:27], v[20:23], v[100:103], v[24:27]\n"
    "buffer_load_dwordx4 v0, s[8:11], s62 offen sc1 lds\n"
    "buffer_load_dwordx4 v0, s[8:11], s62 offen offset:1024 sc1 lds\n"
    "v_mov_b32 v2, 0xff800000\n"
    "v_cndmask_b32 v92, v2, v92, s[72:73]\n"
    "v_cndmask_b32 v93, v2, v93, s[74:75]\n"
    "v_cndmask_b32 v94, v2, v94, s[76:77]\n"
    "v_cndmask_b32 v95, v2, v95, s[78:79]\n"
    "v_cndmask_b32 v72, v2, v72, s[72:73]\n"
    "v_cndmask_b32 v73, v2, v73, s[74:75]\n"
    "v_cndmask_b32 v74, v2, v74, s[76:77]\n"
    "v_cndmask_b32 v75, v2, v75, s[78:79]\n"
    "v_max_f32 v4, v92, v93\n"
    "v_max3_f32 v4, v4, v94, v95\n"
    "v_max3_f32 v4, v4, v64, v65\n"
    "v_max_f32 v5, v84, v85\n"
    "v_max3_f32 v5, v5, v86, v87\n"
    "v_max3_f32 v5, v5, v72, v73\n"
    "v_max3_f32 v5, v5, v74, v75\n"
    "v_max3_f32 v5, v5, v24, v25\n"
    "v_and_b32 v2, 0xf0, v0\n"
    "v_lshrrev_b32 v8, 4, v0\n"
    "v_permlane32_swap_b32 v4, v5\n"
    "v_max_f32 v4, v4, v5\n"
    "v_mov_b32 v5, v4\n"
    "v_and_b32 v8, 48, v8\n"
    "v_xor_b32 v8, v8, v2\n"
    "v_permlane16_swap_b32 v4, v5\n"
    "v_max_f32 v4, v4, v5\n"
    "v_mul_f32 v5, s32, v4\n"
    "v_mul_f32 v4, s32, v4\n"
    "v_lshl_or_b32 v2, v2, 4, v8\n"
    "v_lshrrev_b32 v6, 2, v0\n"
    "v_permlane32_swap_b32 v4, v5\n"
    "v_pk_fma_f32 v[92:93], v[92:93], s[32:33], v[4:5] op_sel:[0,0,0] op_sel_hi:[1,0,0] neg_lo:[0,0,1] neg_hi:[0,0,1]\n"
    "v_pk_fma_f32 v[94:95], v[94:95], s[32:33], v[4:5] op_sel:[0,0,0] op_sel_hi:[1,0,0] neg_lo:[0,0,1] neg_hi:[0,0,1]\n"
    "v_pk_fma_f32 v[64:65], v[64:65], s[32:33], v[4:5] op_sel:[0,0,0] op_sel_hi:[1,0,0] neg_lo:[0,0,1] neg_hi:[0,0,1]\n"
    "v_pk_fma_f32 v[84:85], v[84:85], s[32:33], v[4:5] op_sel:[0,0,1] op_sel_hi:[1,0,1] neg_lo:[0,0,1] neg_hi:[0,0,1]\n"
    "v_pk_fma_f32 v[86:87], v[86:87], s[32:33], v[4:5] op_sel:[0,0,1] op_sel_hi:[1,0,1] neg_lo:[0,0,1] neg_hi:[0,0,1]\n"
    "v_pk_fma_f32 v[72:73], v[72:73], s[32:33], v[4:5] op_sel:[0,0,1] op_sel_hi:[1,0,1] neg_lo:[0,0,1] neg_hi:[0,0,1]\n"
    "v_pk_fma_f32 v[74:75], v[74:75], s[32:33], v[4:5] op_sel:[0,0,1] op_sel_hi:[1,0,1] neg_lo:[0,0,1] neg_hi:[0,0,1]\n"
    "v_pk_fma_f32 v[24:25], v[24:25], s[32:33], v[4:5] op_sel:[0,0,1] op_sel_hi:[1,0,1] neg_lo:[0,0,1] neg_hi:[0,0,1]\n"
    "v_exp_f32 v92, v92\n"
    "v_exp_f32 v93, v93\n"
    "v_exp_f32 v94, v94\n"
    "v_exp_f32 v95, v95\n"
    "v_exp_f32 v64, v64\n"
    "v_exp_f32 v65, v65\n"
    "v_exp_f32 v84, v84\n"
    "rgo3_%=:\n"
    "s_waitcnt vmcnt(3)\n"
    "v_mfma_f32_16x16x32_bf16 v[108:111], v[120:123], v[108:111], 0\n"
    "s_waitcnt vmcnt(2)\n"
    "v_mfma_f32_16x16x32_bf16 v[108:111], v[120:123], v[104:107], v[108:111]\n"
    "s_nop 3\n"
    "v_mov_b32 v0, v0\n"
    "s_nop 2\n"
    "v_add_f32_dpp v108, v108, v108 row_ror:8 row_mask:0xf bank_mask:0xf\n"
    "v_add_f32_dpp v109, v109, v109 row_ror:8 row_mask:0xf bank_mask:0xf\n"
    "v_cmp_u_f32 vcc, v108, v109\n"
    "v_add_f32_dpp v108, v108, v108 row_ror:4 row_mask:0xf bank_mask:0xf\n"
    "v_add_f32_dpp v109, v109, v109 row_ror:4 row_mask:0xf bank_mask:0xf\n"
    "v_cvt_pk_bf16_f32 v110, v108, v109\n"
    "s_cbranch_vccnz slowR3_%=\n"
    "rok3_%=:\n"
    "buffer_store_dword v110, v78, s[8:11], s47 offen sc1\n"
    "v_exp_f32 v85, v85\n"
    "v_exp_f32 v86, v86\n"
    "v_exp_f32 v87, v87\n"
    "v_exp_f32 v72, v72\n"
    "v_exp_f32 v73, v73\n"
    "v_exp_f32 v74, v74\n"
    "v_exp_f32 v75, v75\n"
    "v_exp_f32 v24, v24\n"
    "v_exp_f32 v25, v25\n"
    "v_pk_add_f32 v[66:67], v[92:93], v[94:95]\n"
    "v_pk_add_f32 v[66:67], v[66:67], v[64:65]\n"
    "v_add_f32 v4, v66, v67\n"
    "v_pk_add_f32 v[26:27], v[84:85], v[86:87]\n"
    "v_pk_add_f32 v[26:27], v[26:27], v[72:73]\n"
    "v_pk_add_f32 v[26:27], v[26:27], v[74:75]\n"
    "v_pk_add_f32 v[26:27], v[26:27], v[24:25]\n"
    "v_add_f32 v5, v26, v27\n"
    "v_mov_b64 v[68:69], -1\n"
    "v_mov_b64 v[70:71], -1\n"
    "v_permlane32_swap_b32 v4, v5\n"
    "v_add_f32 v4, v4, v5\n"
    "v_mov_b32 v5, v4\n"
    "v_mov_b32 v117, 0\n"
    "v_mov_b32 v118, 0\n"
    "v_permlane16_swap_b32 v4, v5\n"
    "v_add_f32 v4, v4, v5\n"
    "v_rcp_f32 v4, v4\n"
    "s_nop 0\n"
    "v_mov_b32 v5, v4\n"
    "v_mov_b32 v103, 0\n"
    "s_nop 0\n"
    "v_permlane32_swap_b32 v4, v5\n"
    "v_pk_mul_f32 v[92:93], v[92:93], v[4:5] op_sel:[0,0] op_sel_hi:[1,0]\n"
    "v_pk_mul_f32 v[94:95], v[94:95], v[4:5] op_sel:[0,0] op_sel_hi:[1,0]\n"
    "v_pk_mul_f32 v[64:65], v[64:65], v[4:5] op_sel:[0,0] op_sel_hi:[1,0]\n"
    "v_pk_mul_f32 v[84:85], v[84:85], v[4:5] op_sel:[0,1] op_sel_hi:[1,1]\n"
    "v_pk_mul_f32 v[86:87], v[86:87], v[4:5] op_sel:[0,1] op_sel_hi:[1,1]\n"
    "v_pk_mul_f32 v[72:73], v[72:73], v[4:5] op_sel:[0,1] op_sel_hi:[1,1]\n"
    "v_pk_mul_f32 v[74:75], v[74:75], v[4:5] op_sel:[0,1] op_sel_hi:[1,1]\n"
    "v_pk_mul_f32 v[24:25], v[24:25], v[4:5] op_sel:[0,1] op_sel_hi:[1,1]\n"
    "s_mov_b32 s14, 0x3400000\n"
    "v_cvt_pk_bf16_f32 v28, v84, v85\n"
    "v_cvt_pk_bf16_f32 v29, v86, v87\n"
    "v_cvt_pk_bf16_f32 v30, v72, v73\n"
    "v_cvt_pk_bf16_f32 v31, v74, v75\n"
    "s_add_u32 m0, m0, 0x2000\n"
    "s_add_u32 s62, s45, 0x4000\n"
    "v_mfma_f32_16x16x32_bf16 v[120:123], v[28:31], v[56:59], 0\n"
    "buffer_load_dwordx4 v0, s[8:11], s62 offen sc1 lds\n"
    "v_mfma_f32_16x16x32_bf16 v[108:111], v[28:31], v[60:63], 0\n"
    "v_cvt_pk_bf16_f32 v116, v24, v25\n"
    "v_cvt_pk_bf16_f32 v119, v24, v25\n"
    "v_mfma_f32_16x16x32_bf16 v[104:107], v[28:31], v[48:51], 0\n"
    "buffer_store_dwordx4 v[68:71], v0, s[8:11], s50 offen sc1\n"
    "v_mfma_f32_16x16x32_bf16 v[96:99], v[28:31], v[52:55], 0\n"
    "v_cvt_pk_bf16_f32 v100, v92, v93\n"
    "v_cvt_pk_bf16_f32 v101, v94, v95\n"
    "v_mfma_f32_16x16x32_bf16 v[88:91], v[28:31], v[40:43], 0\n"
    "buffer_store_dwordx4 v[68:71], v0, s[8:11], s50 offen offset:1024 sc1\n"
    "s_add_u32 s61, s46, s49\n"
    "v_mfma_f32_16x16x32_bf16 v[92:95], v[28:31], v[44:47], 0\n"
    "v_cvt_pk_bf16_f32 v102, v64, v65\n"
    "v_mov_b32 v58, v124\n"
    "v_mfma_f32_16x16x32_bf16 v[84:87], v[28:31], v[32:35], 0\n"
    "buffer_store_dword v68, v6, s[8:11], s61 offen sc1\n"
    "v_mfma_f32_16x16x32_bf16 v[80:83], v[28:31], v[36:39], 0\n"
    "buffer_load_dwordx4 v0, s[8:11], s62 offen offset:1024 sc1 lds\n"
    "v_mfma_f32_16x16x16_bf16 v[120:123], v[116:117], v[124:125], v[120:123]\n"
    "v_mov_b32 v62, v125\n"
    "v_mov_b32 v50, v126\n"
    "v_mov_b32 v54, v127\n"
    "v_mfma_f32_16x16x16_bf16 v[108:111], v[118:119], v[124:125], v[108:111]\n"
    "v_mov_b32 v42, v112\n"
    "v_mov_b32 v46, v113\n"
    "v_mov_b32 v34, v114\n"
    "v_mfma_f32_16x16x16_bf16 v[104:107], v[116:117], v[126:127], v[104:107]\n"
    "v_mov_b32 v38, v115\n"
    "v_mfma_f32_16x16x16_bf16 v[96:99], v[118:119], v[126:127], v[96:99]\n"
    "v_cvt_pk_bf16_f32 v72, v120, v108\n"
    "v_cvt_pk_bf16_f32 v68, v121, v109\n"
    "v_cvt_pk_bf16_f32 v64, v122, v110\n"
    "v_mfma_f32_16x16x16_bf16 v[88:91], v[116:117], v[112:113], v[88:91]\n"
    "v_cvt_pk_bf16_f32 v28, v123, v111\n"
    "v_mfma_f32_16x16x16_bf16 v[92:95], v[118:119], v[112:113], v[92:95]\n"
    "v_cvt_pk_bf16_f32 v73, v104, v96\n"
    "v_cvt_pk_bf16_f32 v69, v105, v97\n"
    "v_cvt_pk_bf16_f32 v65, v106, v98\n"
    "v_mfma_f32_16x16x16_bf16 v[84:87], v[116:117], v[114:115], v[84:87]\n"
    "v_cvt_pk_bf16_f32 v29, v107, v99\n"
    "v_mfma_f32_16x16x16_bf16 v[80:83], v[118:119], v[114:115], v[80:83]\n"
    "v_cvt_pk_bf16_f32 v74, v88, v92\n"
    "v_cvt_pk_bf16_f32 v70, v89, v93\n"
    "v_cvt_pk_bf16_f32 v66, v90, v94\n"
    "v_mfma_f32_16x16x32_bf16 v[124:127], v[100:103], v[56:59], 0\n"
    "s_waitcnt vmcnt(6)\n"
    "s_branch join_%=\n"
    "yt0_%=:\n"
    "s_and_b32 s61, s57, 0x1ffff\n"
    "s_lshl_b32 s62, s67, 3\n"
    "s_add_u32 s62, s62, 0x1800000\n"
    "v_lshlrev_b32 v52, 4, v0\n"
    "v_and_b32 v52, 0x1f00, v52\n"
    "yt1_%=:\n"
    "buffer_load_dword v54, v52, s[8:11], s62 offen sc1\n"
    "s_waitcnt vmcnt(0)\n"
    "v_cmp_eq_u32 vcc, -1, v54\n"
    "s_cbranch_vccz yt2_%=\n"
    "s_cmp_eq_u32 s44, 0\n"
    "s_cbranch_scc1 yt2_%=\n"
    "s_sub_u32 s44, s44, 1\n"
    "s_sleep 4\n"
    "s_branch yt1_%=\n"
    "yt2_%=:\n"
    "v_bfe_u32 v53, v0, 1, 6\n"
    "v_lshrrev_b32 v54, 7, v0\n"
    "v_lshl_or_b32 v52, v54, 9, v53\n"
    "v_lshl_or_b32 v53, v54, 10, v53\n"
    "s_lshr_b32 s43, s61, 11\n"
    "s_lshl_b32 s43, s43, 15\n"
    "s_add_u32 s62, s62, s43\n"
    "s_lshr_b32 s43, s61, 3\n"
    "s_and_b32 s43, s43, 0x80\n"
    "s_add_u32 s62, s62, s43\n"
    "s_add_u32 s43, s62, 0x1000\n"
    "yt3_%=:\n"
    "buffer_load_dwordx2 v[34:35], v52, s[8:11], s62 offen sc1\n"
    "buffer_load_dwordx2 v[36:37], v52, s[8:11], s62 offen offset:64 sc1\n"
    "buffer_load_dwordx2 v[38:39], v52, s[8:11], s62 offen offset:256 sc1\n"
    "buffer_load_dwordx2 v[40:41], v52, s[8:11], s62 offen offset:320 sc1\n"
    "buffer_load_dwordx2 v[42:43], v52, s[8:11], s43 offen sc1\n"
    "buffer_load_dwordx2 v[44:45], v52, s[8:11], s43 offen offset:64 sc1\n"
    "buffer_load_dwordx2 v[46:47], v52, s[8:11], s43 offen offset:256 sc1\n"
    "buffer_load_dwordx2 v[48:49], v52, s[8:11], s43 offen offset:320 sc1\n"
    "s_waitcnt vmcnt(6)\n"
    "v_max3_u32 v54, v34, v35, v36\n"
    "s_waitcnt vmcnt(5)\n"
    "v_max3_u32 v54, v54, v37, v38\n"
    "s_waitcnt vmcnt(4)\n"
    "v_max3_u32 v54, v54, v39, v40\n"
    "s_waitcnt vmcnt(3)\n"
    "v_max3_u32 v54, v54, v41, v42\n"
    "s_waitcnt vmcnt(2)\n"
    "v_max3_u32 v54, v54, v43, v44\n"
    "s_waitcnt vmcnt(1)\n"
    "v_max3_u32 v54, v54, v45, v46\n"
    "s_waitcnt vmcnt(0)\n"
    "v_max3_u32 v54, v54, v47, v48\n"
    "v_max_u32 v54, v54, v49\n"
    "v_cmp_eq_u32 vcc, -1, v54\n"
    "s_cbranch_vccz yt4_%=\n"
    "s_cmp_eq_u32 s44, 0\n"
    "s_cbranch_scc1 yt4_%=\n"
    "s_sub_u32 s44, s44, 1\n"
    "s_sleep 4\n"
    "s_branch yt3_%=\n"
    "yt4_%=:\n"
    "v_and_b32 v52, 0x70, v0\n"
    "v_lshrrev_b32 v54, 7, v0\n"
    "v_lshlrev_b32 v54, 2, v54\n"
    "v_lshl_or_b32 v52, v52, 1, v54\n"
    "s_lshl_b32 s62, s61, 4\n"
    "s_add_u32 s62, s62, 0x1000000\n"
    "s_lshl_b32 s43, s68, 1\n"
    "s_add_u32 s62, s62, s43\n"
    "s_add_u32 s43, s62, 0x2000\n"
    "ds_bpermute_b32 v34, v52, v34\n"
    "ds_bpermute_b32 v35, v52, v35\n"
    "ds_bpermute_b32 v36, v52, v36\n"
    "ds_bpermute_b32 v37, v52, v37\n"
    "ds_bpermute_b32 v38, v52, v38\n"
    "ds_bpermute_b32 v39, v52, v39\n"
    "ds_bpermute_b32 v40, v52, v40\n"
    "ds_bpermute_b32 v41, v52, v41\n"
    "ds_bpermute_b32 v42, v52, v42\n"
    "ds_bpermute_b32 v43, v52, v43\n"
    "ds_bpermute_b32 v44, v52, v44\n"
    "ds_bpermute_b32 v45, v52, v45\n"
    "ds_bpermute_b32 v46, v52, v46\n"
    "ds_bpermute_b32 v47, v52, v47\n"
    "ds_bpermute_b32 v48, v52, v48\n"
    "ds_bpermute_b32 v49, v52, v49\n"
    "s_waitcnt lgkmcnt(14)\n"
    "buffer_store_dwordx2 v[34:35], v53, s[8:11], s62 offen sc1\n"
    "s_waitcnt lgkmcnt(12)\n"
    "buffer_store_dwordx2 v[36:37], v53, s[8:11], s43 offen sc1\n"
    "s_waitcnt lgkmcnt(10)\n"
    "buffer_store_dwordx2 v[38:39], v53, s[8:11], s62 offen offset:512 sc1\n"
    "s_waitcnt lgkmcnt(8)\n"
    "buffer_store_dwordx2 v[40:41], v53, s[8:11], s43 offen offset:512 sc1\n"
    "s_waitcnt lgkmcnt(6)\n"
    "buffer_store_dwordx2 v[42:43], v53, s[8:11], s62 offen offset:64 sc1\n"
    "s_waitcnt lgkmcnt(4)\n"
    "buffer_store_dwordx2 v[44:45], v53, s[8:11], s43 offen offset:64 sc1\n"
    "s_waitcnt lgkmcnt(2)\n"
    "buffer_store_dwordx2 v[46:47], v53, s[8:11], s62 offen offset:576 sc1\n"
    "s_waitcnt lgkmcnt(0)\n"
    "buffer_store_dwordx2 v[48:49], v53, s[8:11], s43 offen offset:576 sc1\n"
    "s_bfe_u32 s62, s61, 0x60008\n"
    "s_lshr_b32 s61, s61, 14\n"
    "s_lshl_b32 s61, s61, 7\n"
    "s_add_u32 s61, s61, s62\n"
    "s_add_u32 s61, s61, 0x388040\n"
    "v_mov_b32 v52, 0\n"
    "v_mov_b32 v54, 1\n"
    "s_waitcnt vmcnt(0)\n"
    "s_barrier\n"
    "buffer_store_dword v54, v52, s[8:11], s61 offen sc1\n"
    "s_nop 1\n"
    "s_branch ytend_%=\n"
    "r0chk_%=:\n"
    "s_cmp_eq_u32 s44, 0\n"
    "s_cbranch_scc1 r0ok_%=\n"
    "s_sub_u32 s44, s44, 1\n"
    "s_sleep 4\n"
    "v_lshlrev_b32 v7, 5, v0\n"
    "s_add_u32 s62, s57, s3\n"
    "v_lshrrev_b32 v7, s99, v7\n"
    "buffer_load_dwordx2 v[108:109], v7, s[12:15], s62 offen sc1\n"
    "s_add_u32 s62, s62, s98\n"
    "buffer_load_dwordx2 v[110:111], v7, s[12:15], s62 offen offset:256 sc1\n"
    "s_waitcnt vmcnt(0)\n"
    "v_max3_u32 v6, v108, v109, v110\n"
    "v_max_u32 v6, v6, v111\n"
    "v_cmp_eq_u32 vcc, -1, v6\n"
    "s_cbranch_vccnz r0chk_%=\n"
    "s_branch r0ok_%=\n"
    "huagain_%=:\n"
    "padhuagain0_%=:\n"
    "s_cbranch_execz padhuagain1_%=\n"
    "s_cmp_eq_u32 s44, 0\n"
    "s_cbranch_scc1 hunow_%=\n"
    "s_sub_u32 s44, s44, 1\n"
    "s_sleep 2\n"
    "buffer_load_dwordx4 v[124:127], off, s[8:11], s51 sc1\n"
    "s_branch hupoll_%=\n"
    "hufirst_%=:\n"
    "s_mov_b32 s43, s1\n"
    "padhuagain1_%=:\n"
    "s_cbranch_execz padhuagain2_%=\n"
    "s_waitcnt vmcnt(0)\n"
    "s_branch hugot_%=\n"
    "hunow_%=:\n"
    "s_memrealtime s[80:81]\n"
    "s_waitcnt vmcnt(0) lgkmcnt(0)\n"
    "hulate_%=:\n"
    "s_mov_b32 s43, s80\n"
    "s_mov_b32 s63, 30\n"
    "padhuagain2_%=:\n"
    "s_cbranch_execz padwait00_%=\n"
    "s_branch cload_%=\n"
    "slowI_%=:\n"
    "padslowI0_%=:\n"
    "s_cbranch_execz padslowI1_%=\n"
    "s_waitcnt vmcnt(0) lgkmcnt(0)\n"
    "s_nop 7\n"
    "v_max3_u32 v10, v108, v109, v110\n"
    "v_max3_u32 v10, v10, v111, v72\n"
    "v_max3_u32 v10, v10, v73, v74\n"
    "v_max3_u32 v10, v10, v75, v96\n"
    "v_max3_u32 v10, v10, v97, v98\n"
    "v_max3_u32 v10, v10, v99, v68\n"
    "padslowI1_%=:\n"
    "s_cbranch_execz padslowI2_%=\n"
    "v_max3_u32 v10, v10, v69, v70\n"
    "v_max3_u32 v10, v10, v71, v84\n"
    "v_max3_u32 v10, v10, v85, v86\n"
    "v_max3_u32 v10, v10, v87, v124\n"
    "v_max3_u32 v10, v10, v125, v126\n"
    "v_max3_u32 v10, v10, v127, v112\n"
    "v_max3_u32 v10, v10, v113, v114\n"
    "v_max3_u32 v10, v10, v115, v120\n"
    "padslowI2_%=:\n"
    "s_cbranch_execz padslowI3_%=\n"
    "v_max3_u32 v10, v10, v121, v122\n"
    "v_max_u32 v10, v10, v123\n"
    "v_cmp_eq_u32 vcc, -1, v10\n"
    "s_cbranch_vccz cok_%=\n"
    "s_cmp_eq_u32 s44, 0\n"
    "s_cbranch_scc1 cok_%=\n"
    "s_sub_u32 s44, s44, 1\n"
    "s_or_b32 s97, s97, 0x100\n"
    "padslowI3_%=:\n"
    "s_cbranch_execz padslowI4_%=\n"
    "s_add_u32 s43, s43, 0x4e\n"
    "s_mov_b32 s63, 30\n"
    "ds_read_b128 v[92:95], v0 offset:24640\n"
    "ds_read_b128 v[80:83], v0 offset:24640\n"
    "ds_read_b128 v[76:79], v0 offset:24640\n"
    "ds_read_b128 v[88:91], v0 offset:24640\n"
    "ds_read_b128 v[64:67], v0 offset:24640\n"
    "ds_read_b128 v[8:11], v2\n"
    "padslowI4_%=:\n"
    "s_cbranch_execz dryret_%=\n"
    "ds_read_b128 v[12:15], v2 offset:1024\n"
    "ds_read_b128 v[100:103], v2 offset:16384\n"
    "ds_read_b128 v[104:107], v2 offset:17408\n"
    "ds_read_b128 v[24:27], v2 offset:2048\n"
    "ds_read_b128 v[28:31], v2 offset:3072\n"
    "ds_read_b128 v[16:19], v2 offset:18432\n"
    "ds_read_b128 v[20:23], v2 offset:19456\n"
    "s_branch cload_%=\n"
    "wait0_%=:\n"
    "padwait00_%=:\n"
    "s_cbranch_execz padslowF0_%=\n"
    "s_mov_b64 s[82:83], s[38:39]\n"
    "s_sub_u32 s62, s62, 2\n"
    "wait0a_%=:\n"
    "v_mov_b32 v0, v0\n"
    "s_sub_u32 s62, s62, 2\n"
    "s_cbranch_scc1 go0_%=\n"
    "v_mov_b32 v0, v0\n"
    "s_branch wait0a_%=\n"
    "slowR0_%=:\n"
    "padslowR00_%=:\n"
    "s_cbranch_execz padslowR01_%=\n"
    "s_waitcnt vmcnt(0) lgkmcnt(0)\n"
    "s_nop 7\n"
    "s_cmp_lg_u32 s64, 0\n"
    "s_cbranch_scc1 rok0_%=\n"
    "s_mov_b32 s64, 1\n"
    "slowRa0_%=:\n"
    "s_or_b32 s97, s97, 0x100\n"
    "buffer_load_dwordx4 v[108:111], v0, s[8:11], s50 offen sc1\n"
    "padslowR01_%=:\n"
    "s_cbranch_execz padslowR02_%=\n"
    "buffer_load_dwordx4 v[104:107], v0, s[8:11], s50 offen offset:1024 sc1\n"
    "s_waitcnt vmcnt(1)\n"
    "v_max3_u32 v3, v108, v109, v110\n"
    "s_waitcnt vmcnt(0)\n"
    "v_max3_u32 v3, v3, v111, v104\n"
    "v_max3_u32 v3, v3, v105, v106\n"
    "v_max_u32 v3, v3, v107\n"
    "v_cmp_eq_u32 vcc, -1, v3\n"
    "padslowR02_%=:\n"
    "s_cbranch_execz padslowR03_%=\n"
    "s_cbranch_vccz slowRb0_%=\n"
    "s_cmp_eq_u32 s44, 0\n"
    "s_cbranch_scc1 slowRb0_%=\n"
    "s_sub_u32 s44, s44, 1\n"
    "s_sleep 3\n"
    "s_branch slowRa0_%=\n"
    "slowRb0_%=:\n"
    "s_waitcnt vmcnt(0) lgkmcnt(0)\n"
    "padslowR03_%=:\n"
    "s_cbranch_execz dryret_%=\n"
    "s_nop 7\n"
    "s_branch rgo0_%=\n"
    "slowR1_%=:\n"
    "padslowR10_%=:\n"
    "s_cbranch_execz padslowR11_%=\n"
    "s_waitcnt vmcnt(0) lgkmcnt(0)\n"
    "s_nop 7\n"
    "s_cmp_lg_u32 s64, 0\n"
    "s_cbranch_scc1 rok1_%=\n"
    "s_mov_b32 s64, 1\n"
    "slowRa1_%=:\n"
    "s_or_b32 s97, s97, 0x100\n"
    "buffer_load_dwordx4 v[108:111], v0, s[8:11], s50 offen sc1\n"
    "padslowR11_%=:\n"
    "s_cbranch_execz padslowR12_%=\n"
    "buffer_load_dwordx4 v[104:107], v0, s[8:11], s50 offen offset:1024 sc1\n"
    "s_waitcnt vmcnt(1)\n"
    "v_max3_u32 v3, v108, v109, v110\n"
    "s_waitcnt vmcnt(0)\n"
    "v_max3_u32 v3, v3, v111, v104\n"
    "v_max3_u32 v3, v3, v105, v106\n"
    "v_max_u32 v3, v3, v107\n"
    "v_cmp_eq_u32 vcc, -1, v3\n"
    "padslowR12_%=:\n"
    "s_cbranch_execz padslowR13_%=\n"
    "s_cbranch_vccz slowRb1_%=\n"
    "s_cmp_eq_u32 s44, 0\n"
    "s_cbranch_scc1 slowRb1_%=\n"
    "s_sub_u32 s44, s44, 1\n"
    "s_sleep 3\n"
    "s_branch slowRa1_%=\n"
    "slowRb1_%=:\n"
    "s_waitcnt vmcnt(0) lgkmcnt(0)\n"
    "padslowR13_%=:\n"
    "s_cbranch_execz dryret_%=\n"
    "s_nop 7\n"
    "s_branch rgo1_%=\n"
    "slowR2_%=:\n"
    "padslowR20_%=:\n"
    "s_cbranch_execz padslowR21_%=\n"
    "s_waitcnt vmcnt(0) lgkmcnt(0)\n"
    "s_nop 7\n"
    "s_cmp_lg_u32 s64, 0\n"
    "s_cbranch_scc1 rok2_%=\n"
    "s_mov_b32 s64, 1\n"
    "slowRa2_%=:\n"
    "s_or_b32 s97, s97, 0x100\n"
    "buffer_load_dwordx4 v[108:111], v0, s[8:11], s50 offen sc1\n"
    "padslowR21_%=:\n"
    "s_cbranch_execz padslowR22_%=\n"
    "buffer_load_dwordx4 v[104:107], v0, s[8:11], s50 offen offset:1024 sc1\n"
    "s_waitcnt vmcnt(1)\n"
    "v_max3_u32 v3, v108, v109, v110\n"
    "s_waitcnt vmcnt(0)\n"
    "v_max3_u32 v3, v3, v111, v104\n"
    "v_max3_u32 v3, v3, v105, v106\n"
    "v_max_u32 v3, v3, v107\n"
    "v_cmp_eq_u32 vcc, -1, v3\n"
    "padslowR22_%=:\n"
    "s_cbranch_execz padslowR23_%=\n"
    "s_cbranch_vccz slowRb2_%=\n"
    "s_cmp_eq_u32 s44, 0\n"
    "s_cbranch_scc1 slowRb2_%=\n"
    "s_sub_u32 s44, s44, 1\n"
    "s_sleep 3\n"
    "s_branch slowRa2_%=\n"
    "slowRb2_%=:\n"
    "s_waitcnt vmcnt(0) lgkmcnt(0)\n"
    "padslowR23_%=:\n"
    "s_cbranch_execz dryret_%=\n"
    "s_nop 7\n"
    "s_branch rgo2_%=\n"
    "slowR3_%=:\n"
    "padslowR30_%=:\n"
    "s_cbranch_execz padslowR31_%=\n"
    "s_waitcnt vmcnt(0) lgkmcnt(0)\n"
    "s_nop 7\n"
    "s_cmp_lg_u32 s64, 0\n"
    "s_cbranch_scc1 rok3_%=\n"
    "s_mov_b32 s64, 1\n"
    "slowRa3_%=:\n"
    "s_or_b32 s97, s97, 0x100\n"
    "buffer_load_dwordx4 v[108:111], v0, s[8:11], s50 offen sc1\n"
    "padslowR31_%=:\n"
    "s_cbranch_execz padslowR32_%=\n"
    "buffer_load_dwordx4 v[104:107], v0, s[8:11], s50 offen offset:1024 sc1\n"
    "s_waitcnt vmcnt(1)\n"
    "v_max3_u32 v3, v108, v109, v110\n"
    "s_waitcnt vmcnt(0)\n"
    "v_max3_u32 v3, v3, v111, v104\n"
    "v_max3_u32 v3, v3, v105, v106\n"
    "v_max_u32 v3, v3, v107\n"
    "v_cmp_eq_u32 vcc, -1, v3\n"
    "padslowR32_%=:\n"
    "s_cbranch_execz padslowR33_%=\n"
    "s_cbranch_vccz slowRb3_%=\n"
    "s_cmp_eq_u32 s44, 0\n"
    "s_cbranch_scc1 slowRb3_%=\n"
    "s_sub_u32 s44, s44, 1\n"
    "s_sleep 3\n"
    "s_branch slowRa3_%=\n"
    "slowRb3_%=:\n"
    "s_waitcnt vmcnt(0) lgkmcnt(0)\n"
    "padslowR33_%=:\n"
    "s_cbranch_execz dryret_%=\n"
    "s_nop 7\n"
    "s_branch rgo3_%=\n"
    "epi0_%=:\n"
    "padepi00_%=:\n"
    "s_cbranch_execz padepi01_%=\n"
    "s_nop 7\n"
    "v_cvt_pk_bf16_f32 v69, v98, v94\n"
    "v_cvt_pk_bf16_f32 v49, v99, v95\n"
    "v_cvt_pk_bf16_f32 v74, v88, v84\n"
    "v_cvt_pk_bf16_f32 v54, v89, v85\n"
    "v_cvt_pk_bf16_f32 v70, v90, v86\n"
    "v_cvt_pk_bf16_f32 v50, v91, v87\n"
    "v_cvt_pk_bf16_f32 v75, v80, v44\n"
    "padepi01_%=:\n"
    "s_cbranch_execz padepi02_%=\n"
    "v_cvt_pk_bf16_f32 v55, v81, v45\n"
    "v_cvt_pk_bf16_f32 v71, v82, v46\n"
    "v_cvt_pk_bf16_f32 v51, v83, v47\n"
    "s_xor_b32 s61, s55, 0x1000\n"
    "s_add_u32 s61, s61, 0x18000\n"
    "buffer_store_dwordx4 v[28:31], v76, s[8:11], s61 offen sc1\n"
    "s_add_u32 s61, s55, 0\n"
    "buffer_store_dwordx4 v[72:75], v76, s[8:11], s61 offen sc1\n"
    "padepi02_%=:\n"
    "s_cbranch_execz dryret_%=\n"
    "s_add_u32 s61, s55, 0x8000\n"
    "buffer_store_dwordx4 v[52:55], v76, s[8:11], s61 offen sc1\n"
    "s_add_u32 s61, s55, 0x10000\n"
    "buffer_store_dwordx4 v[68:71], v76, s[8:11], s61 offen sc1\n"
    "s_add_u32 s61, s55, 0x18000\n"
    "buffer_store_dwordx4 v[48:51], v76, s[8:11], s61 offen sc1\n"
    "s_branch epi_%=\n"
    "entry_%=:\n"
    "s_mov_b32 s59, 0x30d40\n"
    "ds_read_b128 v[76:79], v0 offset:29760\n"
    "s_mov_b32 s14, 0\n"
    "s_mov_b32 s63, s42\n"
    "waitH0_%=:\n"
    "s_cmp_eq_u32 s40, 0\n"
    "s_cbranch_scc1 redo_%=\n"
    "waitH1_%=:\n"
    "buffer_load_dword v3, off, s[8:11], s70 sc1\n"
    "s_waitcnt vmcnt(0)\n"
    "v_readfirstlane_b32 s61, v3\n"
    "s_cmp_gt_u32 s61, s63\n"
    "s_cbranch_scc1 redo_%=\n"
    "s_cmp_eq_u32 s44, 0\n"
    "s_cbranch_scc1 redo_%=\n"
    "s_sub_u32 s44, s44, 1\n"
    "s_sleep 2\n"
    "s_branch waitH1_%=\n"
    "waitH_%=:\n"
    "s_add_u32 s63, s42, 4\n"
    "redo_%=:\n"
    "s_waitcnt lgkmcnt(0)\n"
    "s_nop 7\n"
    "s_barrier\n"
    "s_lshl_b32 m0, s67, 1\n"
    "s_add_u32 s62, s45, 0\n"
    "buffer_load_dwordx4 v0, s[8:11], s62 offen sc1 lds\n"
    "buffer_load_dwordx4 v0, s[8:11], s62 offen offset:1024 sc1 lds\n"
    "s_add_u32 m0, m0, 0x2000\n"
    "s_add_u32 s62, s45, 0x4000\n"
    "buffer_load_dwordx4 v0, s[8:11], s62 offen sc1 lds\n"
    "buffer_load_dwordx4 v0, s[8:11], s62 offen offset:1024 sc1 lds\n"
    "v_and_b32 v2, 0xf0, v0\n"
    "v_lshrrev_b32 v8, 4, v0\n"
    "v_and_b32 v8, 48, v8\n"
    "v_xor_b32 v8, v8, v2\n"
    "v_lshl_or_b32 v2, v2, 4, v8\n"
    "s_waitcnt vmcnt(0)\n"
    "s_barrier\n"
    "v_lshlrev_b32 v3, 2, v0\n"
    "ds_read2st64_b32 v[60:61], v3 offset0:0 offset1:16\n"
    "ds_read2st64_b32 v[62:63], v3 offset0:32 offset1:48\n"
    "s_waitcnt lgkmcnt(0)\n"
    "v_max3_u32 v3, v60, v61, v62\n"
    "v_max_u32 v3, v3, v63\n"
    "ds_read_b128 v[60:63], v2\n"
    "ds_read_b128 v[56:59], v2 offset:4096\n"
    "s_waitcnt lgkmcnt(1)\n"
    "v_max_u32 v9, v60, v61\n"
    "v_max3_u32 v9, v9, v62, v63\n"
    "s_waitcnt lgkmcnt(0)\n"
    "v_max3_u32 v9, v9, v56, v57\n"
    "v_max3_u32 v9, v9, v58, v59\n"
    "v_cmp_eq_u32 vcc, -1, v9\n"
    "v_max_u32 v3, v3, v9\n"
    "v_cmp_eq_u32 vcc, -1, v3\n"
    "s_cbranch_vccz pbgo_%=\n"
    "s_cmp_eq_u32 s59, 0\n"
    "s_cbranch_scc1 pbgo_%=\n"
    "redo1_%=:\n"
    "s_sub_u32 s59, s59, 1\n"
    "s_sleep 4\n"
    "s_branch waitH0_%=\n"
    "slowE_%=:\n"
    "s_cmp_eq_u32 s59, 0\n"
    "s_cbranch_scc1 eok_%=\n"
    "s_branch slowN1_%=\n"
    "slowF_%=:\n"
    "padslowF0_%=:\n"
    "s_cbranch_execz padslowF1_%=\n"
    "s_waitcnt vmcnt(0) lgkmcnt(0)\n"
    "s_nop 7\n"
    "slowF1_%=:\n"
    "s_cmp_eq_u32 s44, 0\n"
    "s_cbranch_scc1 flok_%=\n"
    "s_sub_u32 s44, s44, 1\n"
    "s_sleep 3\n"
    "buffer_load_dwordx2 v[6:7], v10, s[8:11], s54 offen sc1\n"
    "padslowF1_%=:\n"
    "s_cbranch_execz padyck0_%=\n"
    "s_waitcnt vmcnt(0)\n"
    "v_cmp_ne_u16 vcc, s33, v6\n"
    "s_cbranch_vccnz slowF1_%=\n"
    "s_branch flok_%=\n"
    "slowN_%=:\n"
    "s_cmp_eq_u32 s59, 0\n"
    "s_cbranch_scc1 pbdone_%=\n"
    "slowN1_%=:\n"
    "s_mov_b32 s14, 0\n"
    "s_mov_b32 s63, 0\n"
    "s_waitcnt vmcnt(0) lgkmcnt(0)\n"
    "s_nop 7\n"
    "s_nop 0\n"
    "ds_read_b128 v[76:79], v0 offset:29760\n"
    "s_waitcnt vmcnt(0)\n"
    "s_branch redo1_%=\n"
    "rwl1_%=:\n"
    "buffer_store_dwordx4 v[100:103], v0, s[20:23], s57 offen\n"
    "s_nop 1\n"
    "s_branch rwadv_%=\n"
    "rwchk_%=:\n"
    "s_cmp_eq_u32 s44, 0\n"
    "s_cbranch_scc1 rwok_%=\n"
    "s_sub_u32 s44, s44, 1\n"
    "s_sleep 4\n"
    "v_lshlrev_b32 v7, 5, v0\n"
    "s_add_u32 s62, s57, s3\n"
    "v_lshrrev_b32 v7, s99, v7\n"
    "buffer_load_dwordx2 v[72:73], v7, s[12:15], s62 offen sc1\n"
    "s_add_u32 s62, s62, s98\n"
    "buffer_load_dwordx2 v[74:75], v7, s[12:15], s62 offen offset:256 sc1\n"
    "s_waitcnt vmcnt(0)\n"
    "v_max3_u32 v6, v72, v73, v74\n"
    "v_max_u32 v6, v6, v75\n"
    "v_cmp_eq_u32 vcc, -1, v6\n"
    "s_cbranch_vccnz rwchk_%=\n"
    "s_branch rwok_%=\n"
    "yck_%=:\n"
    "padyck0_%=:\n"
    "s_cbranch_execz padyck1_%=\n"
    "s_cmp_lt_u32 s42, 3\n"
    "s_cbranch_scc1 yok_%=\n"
    "v_bfe_u32 v4, v0, 1, 6\n"
    "v_and_b32 v2, 0x380, v0\n"
    "v_or_b32 v4, v4, v2\n"
    "s_mov_b32 s61, 0x388040\n"
    "buffer_load_dwordx2 v[2:3], v4, s[8:11], s61 offen sc1\n"
    "s_waitcnt vmcnt(0)\n"
    "padyck1_%=:\n"
    "s_cbranch_execz dryret_%=\n"
    "v_and_b32 v2, v2, v3\n"
    "v_cmp_ne_u32 vcc, 1, v2\n"
    "s_cbranch_vccnz yok_%=\n"
    "s_mov_b32 s98, 0x100\n"
    "s_mov_b32 s99, 6\n"
    "s_mov_b32 s3, 0x1000000\n"
    "s_branch yok_%=\n"
    "rel1_%=:\n"
    "padrel10_%=:\n"
    "s_cbranch_execz dryret_%=\n"
    "buffer_store_dwordx4 v[92:95], v0, s[20:23], s57 offen\n"
    "s_nop 1\n"
    "s_branch readv_%=\n"
    "rechk_%=:\n"
    "s_cmp_eq_u32 s44, 0\n"
    "s_cbranch_scc1 reok_%=\n"
    "s_sub_u32 s44, s44, 1\n"
    "s_sleep 4\n"
    "v_lshlrev_b32 v7, 5, v0\n"
    "s_add_u32 s62, s57, s3\n"
    "v_lshrrev_b32 v7, s99, v7\n"
    "buffer_load_dwordx2 v[96:97], v7, s[12:15], s62 offen sc1\n"
    "s_add_u32 s62, s62, s98\n"
    "buffer_load_dwordx2 v[98:99], v7, s[12:15], s62 offen offset:256 sc1\n"
    "s_waitcnt vmcnt(0)\n"
    "v_max3_u32 v6, v96, v97, v98\n"
    "v_max_u32 v6, v6, v99\n"
    "v_cmp_eq_u32 vcc, -1, v6\n"
    "s_cbranch_vccnz rechk_%=\n"
    "s_branch reok_%=\n"
    "end_%=:\n"
    "s_endpgm\n"
    "Lcode_end:\n"
    :
    : "{s[8:9]}"(ws), "{s[72:73]}"(wq), "{s[74:75]}"(wv), "{s[76:77]}"(wk), "{s[78:79]}"(wo), "{s41}"(w), "{s59}"(w * 262144 + h * 256),
      "{s60}"(blockIdx.x)
    : "v255", "a255", "s99", "vcc", "scc", "memory");
}

extern "C" void kernel_launch(void* const* d_in, const int* in_sizes, int n_in, void* d_out, int out_size,
                              void* d_ws, size_t ws_size, hipStream_t stream) {
  char* ws = (char*)(((size_t)d_ws + (2u << 20) - 1) & ~(size_t)((2u << 20) - 1));
  k_pre<<<256, 512, 0, stream>>>((const u16*)d_in[1], (const u16*)d_in[2], (const u16*)d_in[3], (const u16*)d_in[7], ws, (const u16*)d_in[0], (u16*)d_out,
                                 (const char*)d_in[11], (const char*)d_in[4]);
  k_main<<<256, 256, 0, stream>>>(ws, (const u16*)d_in[0], (const u16*)d_in[4], (const u16*)d_in[5], (const u16*)d_in[8],
                                  (const u16*)d_in[9], (const u16*)d_in[10], (const u16*)d_in[11]);
}
```
